```python
import torch

def _softmax(x, axis=-1):
    return torch.softmax(x, dim=axis)
def _gelu(x):
    return torch.nn.functional.gelu(x, approximate="tanh")
def _silu(x):
    return torch.nn.functional.silu(x)
def _sigmoid(x):
    return torch.sigmoid(x)
def _layernorm(x, axis=-1, eps=1e-5):
    m = x.mean(dim=axis, keepdim=True)
    v = x.var(dim=axis, keepdim=True, unbiased=False)
    return (x - m) / torch.sqrt(v + eps)
def _scatter_add(tgt, idx, src, axis=0):
    return tgt.clone().index_add_(axis, idx, src)
def _segment_sum(src, seg, n):
    out = torch.zeros((n,) + src.shape[1:], dtype=src.dtype)
    return out.index_add_(0, seg, src)
def _pad(x, pw):
    flat = tuple(v for pair in reversed(pw) for v in pair)
    return torch.nn.functional.pad(x, flat)

kernel_name = 'standalone__controlnet__diffusion'

def setup_inputs():

    _w = (lambda a: (lambda t: t.to(torch.bfloat16) if t.is_floating_point() else t)(torch.as_tensor(a)))
    B, HW, C = 2, 32, 512
    sc = C ** -0.5
    return {
        "x_frozen": _w((lambda *s: torch.randn(*s, dtype=torch.bfloat16) * 0.4)(B, HW, HW, C)),
        "x": _w((lambda *s: torch.randn(*s, dtype=torch.bfloat16) * 0.8)(B, HW, HW, C)),
        "c": _w((lambda *s: torch.randn(*s, dtype=torch.bfloat16) * 1.6)(B, HW, HW, C)),
        "w_z1": _w((lambda *s: torch.randn(*s, dtype=torch.bfloat16) * 1.6)(C, C) * sc),
        "w_z2": _w((lambda *s: torch.randn(*s, dtype=torch.bfloat16) * 0.8)(C, C) * sc),
        "w_conv": _w((lambda *s: torch.randn(*s, dtype=torch.bfloat16) * 0.8)(3, 3, C, C) * sc / 3.0),
    }

def reference(x_frozen, x, c, w_z1, w_z2, w_conv):
    B, H, W, C = x.shape
    h_in = x + torch.einsum("bhwc,cd->bhwd", c, w_z1)
    h = _silu(_layernorm(h_in, axis=-1))
    hp = _pad(h, ((0, 0), (1, 1), (1, 1), (0, 0)))
    acc = h * 0.0
    for di in range(3):
        for dj in range(3):
            acc = acc + torch.einsum("bhwc,cd->bhwd", hp[:, di:di + H, dj:dj + W, :], w_conv[di, dj])
    h = h_in + acc
    return x_frozen + torch.einsum("bhwc,cd->bhwd", h, w_z2)
```

```cpp
#include <hip/hip_runtime.h>
#include <hip/hip_bf16.h>
typedef unsigned short u16;
__device__ __forceinline__ float b2f(u16 v){ return __builtin_bit_cast(float, (unsigned)v<<16); }
__device__ __forceinline__ u16 f2b(float f){ __hip_bfloat16 b = __float2bfloat16(f); return __builtin_bit_cast(u16,b); }

__global__ void __launch_bounds__(256) k1(const u16* x, const u16* c, const u16* w1, float* hin, u16* h){
  __shared__ float cs[512]; __shared__ float red[2][4];
  int m = blockIdx.x, t = threadIdx.x;
  cs[t] = b2f(c[m*512+t]); cs[t+256] = b2f(c[m*512+t+256]);
  __syncthreads();
  float a0=0,a1=0;
  for(int k=0;k<512;k++){ a0 += cs[k]*b2f(w1[k*512+t]); a1 += cs[k]*b2f(w1[k*512+t+256]); }
  a0 += b2f(x[m*512+t]); a1 += b2f(x[m*512+t+256]);
  float s = a0+a1;
  for(int o=32;o>0;o>>=1) s += __shfl_xor(s,o);
  if((t&63)==0) red[0][t>>6]=s;
  __syncthreads();
  float mean = (red[0][0]+red[0][1]+red[0][2]+red[0][3])*(1.f/512);
  float d0=a0-mean,d1=a1-mean; float q=d0*d0+d1*d1;
  for(int o=32;o>0;o>>=1) q += __shfl_xor(q,o);
  if((t&63)==0) red[1][t>>6]=q;
  __syncthreads();
  float var = (red[1][0]+red[1][1]+red[1][2]+red[1][3])*(1.f/512);
  float r = rsqrtf(var+1e-5f);
  float y0=d0*r,y1=d1*r;
  hin[m*512+t]=a0; hin[m*512+t+256]=a1;
  h[m*512+t]=f2b(y0/(1.f+__expf(-y0))); h[m*512+t+256]=f2b(y1/(1.f+__expf(-y1)));
}
__global__ void __launch_bounds__(256) k2(const u16* xf, const float* hin, const u16* h, const u16* wc, const u16* w2, u16* out){
  __shared__ float hs[512];
  int m = blockIdx.x, t = threadIdx.x;
  int b = m>>10, y=(m>>5)&31, xx=m&31;
  float a0=0,a1=0;
  for(int di=0;di<3;di++) for(int dj=0;dj<3;dj++){
    int yy=y+di-1, x2=xx+dj-1;
    if(yy<0||yy>31||x2<0||x2>31) continue;
    int mm = (b<<10)+(yy<<5)+x2;
    __syncthreads();
    hs[t]=b2f(h[mm*512+t]); hs[t+256]=b2f(h[mm*512+t+256]);
    __syncthreads();
    const u16* w = wc + (size_t)(di*3+dj)*512*512;
    for(int k=0;k<512;k++){ a0 += hs[k]*b2f(w[k*512+t]); a1 += hs[k]*b2f(w[k*512+t+256]); }
  }
  a0 += hin[m*512+t]; a1 += hin[m*512+t+256];
  __syncthreads();
  hs[t]=b2f(f2b(a0)); hs[t+256]=b2f(f2b(a1));
  __syncthreads();
  a0=0;a1=0;
  for(int k=0;k<512;k++){ a0 += hs[k]*b2f(w2[k*512+t]); a1 += hs[k]*b2f(w2[k*512+t+256]); }
  out[m*512+t]=f2b(a0+b2f(xf[m*512+t])); out[m*512+t+256]=f2b(a1+b2f(xf[m*512+t+256]));
}
extern "C" void kernel_launch(void* const* d_in, const int* in_sizes, int n_in, void* d_out, int out_size, void* d_ws, size_t ws_size, hipStream_t stream){
  const u16* xf=(const u16*)d_in[0]; const u16* x=(const u16*)d_in[1]; const u16* c=(const u16*)d_in[2];
  const u16* w1=(const u16*)d_in[3]; const u16* w2=(const u16*)d_in[4]; const u16* wc=(const u16*)d_in[5];
  float* hin=(float*)d_ws; u16* h=(u16*)((char*)d_ws + 2048*512*4);
  k1<<<2048,256,0,stream>>>(x,c,w1,hin,h);
  k2<<<2048,256,0,stream>>>(xf,hin,h,wc,w2,(u16*)d_out);
}
```

```cpp
#include <hip/hip_runtime.h>

__global__ void __launch_bounds__(256, 1) cn_fused(const void* xf, const void* x, const void* c, const void* w1, const void* w2,
                                                   const void* wc, void* out, void* ws) {
#if defined(__HIP_DEVICE_COMPILE__) && __HIP_DEVICE_COMPILE__
  __shared__ __attribute__((aligned(16))) char lds[163840];
  asm volatile(
"s_load_dwordx16 s[8:23], %[ka], 0x0\n"
"s_getpc_b64 s[86:87]\n"
".Lcn_L_pc:\n"
"v_lshlrev_b32_e32 v247, 6, %[tid]\n"
"s_bfe_u32 s85, %[wg], 0x10003\n"
"s_lshl_b32 s85, s85, 14\n"
"v_add_u32_e32 v247, s85, v247\n"
"v_min_u32_e32 v247, .Lcn_L_end-.Lcn_L_pc, v247\n"
"global_load_dword v136, v247, s[86:87]\n"
"v_lshrrev_b32_e32 v240, 6, %[tid]\n"
"v_and_b32_e32 v241, 63, %[tid]\n"
"v_readfirstlane_b32 s56, v240\n"
"s_and_b32 s57, %[wg], 3\n"
"s_bfe_u32 s59, %[wg], 0x10002\n"
"s_bfe_u32 s61, %[wg], 0x20003\n"
"s_lshr_b32 s58, %[wg], 5\n"
"s_lshl2_add_u32 s62, s59, s61\n"
"s_lshl2_add_u32 s63, s58, s57\n"
"s_mul_i32 s64, s56, 40960\n"
"v_lshrrev_b32_e32 v242, 4, v241\n"
"v_and_b32_e32 v243, 15, v241\n"
"v_lshlrev_b32_e32 v1, 4, v241\n"
"v_and_b32_e32 v244, 7, v241\n"
"v_lshrrev_b32_e32 v245, 3, v241\n"
"v_xor_b32_e32 v246, v244, v242\n"
"v_lshlrev_b32_e32 v246, 4, v246\n"
"v_lshl_add_u32 v176, v245, 11, v246\n"
"v_xor_b32_e32 v177, 64, v176\n"
"v_add_u32_e32 v177, 15360, v177\n"
"v_lshlrev_b32_e32 v246, 1, v242\n"
"v_xor_b32_e32 v246, v244, v246\n"
"v_lshlrev_b32_e32 v246, 4, v246\n"
"v_lshrrev_b32_e32 v247, 5, v241\n"
"v_lshl_add_u32 v13, v247, 13, v246\n"
"v_and_b32_e32 v247, 3, v245\n"
"v_lshl_add_u32 v13, v247, 10, v13\n"
"v_add_u32_e32 v14, 15360, v13\n"
"v_lshl_add_u32 v3, v245, 10, v246\n"
"s_lshr_b32 s88, s56, 1\n"
"s_lshl1_add_u32 s4, s63, s88\n"
"s_lshl_b32 s4, s4, 5\n"
"s_and_b32 s87, s56, 1\n"
"s_add_u32 s4, s4, s87\n"
"s_lshl_b32 s4, s4, 10\n"
"s_lshl3_add_u32 s5, s88, s87\n"
"s_lshl_b32 s5, s5, 12\n"
"s_lshl_b32 s86, s62, 7\n"
"s_add_u32 s5, s5, s86\n"
"s_add_u32 s88, s86, s4\n"
"s_lshl1_add_u32 s6, s61, s58\n"
"s_lshl_b32 s6, s6, 16\n"
"v_add_u32_e32 v192, s5, v13\n"
"v_add_u32_e32 v194, s5, v14\n"
"v_add_u32_e32 v196, s4, v176\n"
"v_add_u32_e32 v198, s4, v177\n"
"s_lshl2_add_u32 s81, s63, s56\n"
"s_lshl3_add_u32 s65, s81, s62\n"
"s_lshl_b32 s81, s65, 11\n"
"s_lshr_b32 s77, s58, 2\n"
"s_and_b32 s60, s58, 3\n"
"s_lshl3_add_u32 s85, s77, s60\n"
"s_lshl2_add_u32 s85, s85, 12\n"
"s_lshr_b32 s89, %[wg], 3\n"
"s_lshl2_add_u32 s89, s89, s56\n"
"v_and_b32_e32 v21, 0x200, v1\n"
"v_add_u32_e32 v21, v21, v1\n"
"s_lshl_b32 s87, s59, 9\n"
"v_add_u32_e32 v21, s87, v21\n"
"s_add_u32 m0, s64, 32768\n"
"s_waitcnt lgkmcnt(0)\n"
"s_mov_b32 s32, s14\n"
"s_and_b32 s33, s15, 0xffff\n"
"s_mov_b32 s34, 524288\n"
"s_mov_b32 s35, 0x20000\n"
"s_mov_b32 s28, s10\n"
"s_and_b32 s29, s11, 0xffff\n"
"s_mov_b32 s30, 2097152\n"
"s_mov_b32 s31, 0x20000\n"
"buffer_load_dwordx4 v176, s[28:31], s88 offen lds\n"
"buffer_load_dwordx4 v177, s[28:31], s88 offen offset:1024 lds\n"
"s_sub_u32 s24, s12, 2048\n"
"s_subb_u32 s25, s13, 0\n"
"s_and_b32 s25, s25, 0xffff\n"
"s_mov_b32 s26, 2099200\n"
"s_mov_b32 s27, 0x20000\n"
"s_load_dwordx4 s[68:71], s[22:23], s85\n"
"s_and_b32 s6, s6, 0x7ffff\n"
"s_add_u32 m0, s64, 0\n"
"s_lshr_b32 s99, s6, 9\n"
"buffer_load_dwordx4 v192, s[32:35], s6 offen lds\n"
"buffer_load_dwordx4 v194, s[32:35], s6 offen offset:1024 lds\n"
"buffer_load_dwordx4 v196, s[24:27], s99 offen offset:2048 lds\n"
"buffer_load_dwordx4 v198, s[24:27], s99 offen offset:3072 lds\n"
"s_add_u32 s6, s6, 0x10000\n"
"s_and_b32 s6, s6, 0x7ffff\n"
"s_add_u32 m0, s64, 4096\n"
"s_lshr_b32 s99, s6, 9\n"
"buffer_load_dwordx4 v192, s[32:35], s6 offen lds\n"
"buffer_load_dwordx4 v194, s[32:35], s6 offen offset:1024 lds\n"
"buffer_load_dwordx4 v196, s[24:27], s99 offen offset:2048 lds\n"
"buffer_load_dwordx4 v198, s[24:27], s99 offen offset:3072 lds\n"
"s_add_u32 s6, s6, 0x10000\n"
"s_mov_b32 s52, s22\n"
"s_and_b32 s53, s23, 0xffff\n"
"s_mov_b32 s54, 12582912\n"
"s_mov_b32 s55, 0x20000\n"
"s_mov_b32 s36, s18\n"
"s_and_b32 s37, s19, 0xffff\n"
"s_mov_b32 s38, 4718592\n"
"s_mov_b32 s39, 0x20000\n"
"s_lshl2_add_u32 s78, s57, s56\n"
"s_lshl_b32 s78, s78, 15\n"
"s_add_u32 s78, s78, s86\n"
"v_add_u32_e32 v4, 7168, v3\n"
"v_add_u32_e32 v5, 14336, v3\n"
"v_add_u32_e32 v6, 21504, v3\n"
"s_and_b32 s101, s89, 63\n"
"s_lshl_b32 s101, s101, 11\n"
"s_lshl_b32 s87, s57, 17\n"
"s_add_u32 s101, s101, s87\n"
"v_and_b32_e32 v244, 3, v245\n"
"v_lshlrev_b32_e32 v244, 5, v244\n"
"v_lshrrev_b32_e32 v246, 2, v241\n"
"v_lshl_add_u32 v244, v246, 7, v244\n"
"v_and_b32_e32 v246, 3, v241\n"
"v_lshlrev_b32_e32 v247, 3, v246\n"
"v_xor_b32_e32 v185, v244, v247\n"
"v_lshlrev_b32_e32 v247, 4, v246\n"
"v_xor_b32_e32 v247, v244, v247\n"
"v_mov_b32_e32 v9, v247\n"
"s_lshl_b32 s75, s56, 14\n"
"v_xor_b32_e32 v178, 0, v185\n"
"v_add_u32_e32 v188, 81920, v178\n"
"v_xor_b32_e32 v179, 32, v185\n"
"v_add_u32_e32 v189, 81920, v179\n"
"v_xor_b32_e32 v180, 64, v185\n"
"v_add_u32_e32 v190, 81920, v180\n"
"v_xor_b32_e32 v181, 96, v185\n"
"v_add_u32_e32 v191, 81920, v181\n"
"v_lshrrev_b32_e32 v244, 1, v243\n"
"v_lshlrev_b32_e32 v244, 4, v244\n"
"v_lshl_add_u32 v244, v243, 7, v244\n"
"v_lshlrev_b32_e32 v246, 4, v242\n"
"v_xor_b32_e32 v246, v244, v246\n"
"v_add_u32_e32 v182, s64, v246\n"
"v_xor_b32_e32 v246, 64, v246\n"
"v_add_u32_e32 v183, s64, v246\n"
"v_lshlrev_b32_e32 v246, 3, v242\n"
"v_xor_b32_e32 v184, v244, v246\n"
"v_mov_b32_e32 v247, 2130706432\n"
"v_cmp_gt_u32_e32 vcc, 16, v241\n"
"s_nop 1\n"
"v_cndmask_b32_e32 v27, v247, v1, vcc\n"
"v_cmp_gt_u32_e32 vcc, 1, v241\n"
"s_nop 1\n"
"v_cndmask_b32_e32 v28, v247, v1, vcc\n"
"v_add_u32_e32 v244, 524032, v1\n"
"v_cmp_eq_u32_e64 s[72:73], 1, v242\n"
"s_nop 1\n"
"v_cndmask_b32_e64 v25, v27, v244, s[72:73]\n"
"s_mov_b32 s82, 0\n"
"s_lshl_b32 s83, s77, 20\n"
"s_add_u32 s83, s83, 2097152\n"
"s_add_u32 s28, s52, s83\n"
"s_addc_u32 s29, s53, 0\n"
"s_mov_b32 s30, 1048576\n"
"s_lshl_b32 s13, s56, 12\n"
"v_add_u32_e32 v2, 15360, v1\n"
"s_lshl_b32 s87, s56, 5\n"
"v_xor_b32_e32 v244, s87, v185\n"
"v_add_u32_e32 v7, 32768, v244\n"
"v_add_u32_e32 v8, 98304, v244\n"
"v_add_u32_e32 v10, s13, v1\n"
"v_add_u32_e32 v11, 65536, v10\n"
"v_mov_b32_e32 v244, 1048576\n"
"v_subrev_u32_e32 v19, 16, v1\n"
"v_add_u32_e32 v20, 16, v1\n"
"v_cmp_eq_u32_e32 vcc, 0, v243\n"
"s_nop 1\n"
"v_cndmask_b32_e32 v19, v19, v244, vcc\n"
"v_cmp_eq_u32_e32 vcc, 15, v243\n"
"s_nop 1\n"
"v_cndmask_b32_e32 v20, v20, v244, vcc\n"
"s_lshl_b32 s87, s60, 4\n"
"s_sub_u32 s87, s87, 2\n"
"v_add_u32_e32 v246, s87, v241\n"
"s_lshr_b32 s85, s56, 1\n"
"s_lshl1_add_u32 s85, s57, s85\n"
"s_lshl_b32 s84, s77, 9\n"
"s_add_u32 s85, s85, s84\n"
"v_lshl_add_u32 v17, v246, 3, s85\n"
"s_lshl4_add_u32 s87, s63, s56\n"
"s_lshl_b32 s87, s87, 1\n"
"s_sub_u32 s87, s87, 80\n"
"v_and_b32_e32 v245, 1, v241\n"
"v_lshrrev_b32_e32 v244, 1, v241\n"
"v_lshl_add_u32 v244, v244, 3, v245\n"
"v_add_u32_e32 v244, s87, v244\n"
"v_cmp_lt_u32_e32 vcc, 19, v241\n"
"s_nop 1\n"
"v_cndmask_b32_e32 v17, v17, v244, vcc\n"
"v_cndmask_b32_e64 v246, v246, 0, vcc\n"
"s_lshr_b32 s87, s81, 11\n"
"s_xor_b32 s85, s87, 32\n"
"v_writelane_b32 v17, s85, 28\n"
"s_xor_b32 s85, s87, 64\n"
"v_writelane_b32 v17, s85, 29\n"
"s_xor_b32 s85, s87, 96\n"
"v_writelane_b32 v17, s85, 30\n"
"v_lshlrev_b32_e32 v17, 8, v17\n"
"v_add_u32_e32 v17, 1048580, v17\n"
"v_add_u32_e32 v244, 524288, v17\n"
"s_mov_b64 vcc, 0xc0003\n"
"v_cndmask_b32_e32 v17, v17, v244, vcc\n"
"v_cmp_gt_u32_e64 s[96:97], 64, v246\n"
"s_and_b64 s[96:97], s[96:97], 0x7fffffff\n"
"s_nop 0\n"
"v_cndmask_b32_e64 v17, v247, v17, s[96:97]\n"
"s_and_b32 s96, s96, 0xfffff\n"
"s_and_b32 s6, s6, 0x7ffff\n"
"s_add_u32 m0, s64, 8192\n"
"s_lshr_b32 s99, s6, 9\n"
"buffer_load_dwordx4 v192, s[32:35], s6 offen lds\n"
"buffer_load_dwordx4 v194, s[32:35], s6 offen offset:1024 lds\n"
"buffer_load_dwordx4 v196, s[24:27], s99 offen offset:2048 lds\n"
"buffer_load_dwordx4 v198, s[24:27], s99 offen offset:3072 lds\n"
"s_add_u32 s6, s6, 0x10000\n"
"s_waitcnt lgkmcnt(0)\n"
"s_add_u32 s65, s68, 1\n"
"s_add_u32 s66, s69, 1\n"
"s_add_u32 s67, s70, 1\n"
"s_cmp_lt_u32 s57, 2\n"
"s_cselect_b32 s65, s65, s67\n"
"v_mov_b32_e32 v251, s66\n"
"v_mov_b32_e32 v249, s65\n"
"v_cndmask_b32_e64 v249, v251, v249, s[72:73]\n"
"s_waitcnt vmcnt(12)\n"
"v_xor_b32_e32 v186, 0, v184\n"
"v_add_u32_e32 v186, s64, v186\n"
"ds_read_b64 v[160:161], v186 offset:32768\n"
"v_xor_b32_e32 v186, 32, v184\n"
"v_add_u32_e32 v186, s64, v186\n"
"ds_read_b64 v[162:163], v186 offset:32768\n"
"v_xor_b32_e32 v186, 64, v184\n"
"v_add_u32_e32 v186, s64, v186\n"
"ds_read_b64 v[164:165], v186 offset:32768\n"
"v_xor_b32_e32 v186, 96, v184\n"
"v_add_u32_e32 v186, s64, v186\n"
"ds_read_b64 v[166:167], v186 offset:32768\n"
"s_waitcnt lgkmcnt(3)\n"
"v_lshlrev_b32_e32 v224, 16, v160\n"
"v_and_b32_e32 v225, 0xffff0000, v160\n"
"v_lshlrev_b32_e32 v226, 16, v161\n"
"v_and_b32_e32 v227, 0xffff0000, v161\n"
"s_waitcnt lgkmcnt(2)\n"
"v_lshlrev_b32_e32 v228, 16, v162\n"
"v_and_b32_e32 v229, 0xffff0000, v162\n"
"v_lshlrev_b32_e32 v230, 16, v163\n"
"v_and_b32_e32 v231, 0xffff0000, v163\n"
"s_waitcnt lgkmcnt(1)\n"
"v_lshlrev_b32_e32 v232, 16, v164\n"
"v_and_b32_e32 v233, 0xffff0000, v164\n"
"v_lshlrev_b32_e32 v234, 16, v165\n"
"v_and_b32_e32 v235, 0xffff0000, v165\n"
"s_waitcnt lgkmcnt(0)\n"
"v_lshlrev_b32_e32 v236, 16, v166\n"
"v_and_b32_e32 v237, 0xffff0000, v166\n"
"v_lshlrev_b32_e32 v238, 16, v167\n"
"v_and_b32_e32 v239, 0xffff0000, v167\n"
"s_waitcnt vmcnt(8)\n"
"s_barrier\n"
"ds_read_b128 v[160:163], v182 offset:2048\n"
"ds_read_b64_tr_b16 v[32:33], v178 offset:0\n"
"s_and_b32 s6, s6, 0x7ffff\n"
"ds_read_b64_tr_b16 v[34:35], v178 offset:40960\n"
"ds_read_b64_tr_b16 v[36:37], v179 offset:0\n"
"s_add_u32 m0, s64, 12288\n"
"ds_read_b64_tr_b16 v[38:39], v179 offset:40960\n"
"ds_read_b64_tr_b16 v[40:41], v180 offset:0\n"
"s_lshr_b32 s99, s6, 9\n"
"ds_read_b64_tr_b16 v[42:43], v180 offset:40960\n"
"ds_read_b64_tr_b16 v[44:45], v181 offset:0\n"
"buffer_load_dwordx4 v192, s[32:35], s6 offen lds\n"
"ds_read_b64_tr_b16 v[46:47], v181 offset:40960\n"
"ds_read_b128 v[164:167], v183 offset:2048\n"
"buffer_load_dwordx4 v194, s[32:35], s6 offen offset:1024 lds\n"
"ds_read_b64_tr_b16 v[48:49], v188 offset:0\n"
"ds_read_b64_tr_b16 v[50:51], v188 offset:40960\n"
"buffer_load_dwordx4 v196, s[24:27], s99 offen offset:2048 lds\n"
"ds_read_b64_tr_b16 v[52:53], v189 offset:0\n"
"ds_read_b64_tr_b16 v[54:55], v189 offset:40960\n"
"buffer_load_dwordx4 v198, s[24:27], s99 offen offset:3072 lds\n"
"ds_read_b64_tr_b16 v[56:57], v190 offset:0\n"
"ds_read_b64_tr_b16 v[58:59], v190 offset:40960\n"
"s_add_u32 s6, s6, 0x10000\n"
"ds_read_b64_tr_b16 v[60:61], v191 offset:0\n"
"ds_read_b64_tr_b16 v[62:63], v191 offset:40960\n"
"s_waitcnt vmcnt(8)\n"
"s_barrier\n"
"ds_read_b128 v[168:171], v182 offset:6144\n"
"ds_read_b64_tr_b16 v[64:65], v178 offset:4096\n"
"s_waitcnt lgkmcnt(11)\n"
"v_mfma_f32_16x16x32_bf16 v[224:227], v[32:35], v[160:163], v[224:227]\n"
"s_and_b32 s6, s6, 0x7ffff\n"
"ds_read_b64_tr_b16 v[66:67], v178 offset:45056\n"
"ds_read_b64_tr_b16 v[68:69], v179 offset:4096\n"
"v_mfma_f32_16x16x32_bf16 v[228:231], v[36:39], v[160:163], v[228:231]\n"
"s_add_u32 m0, s64, 16384\n"
"ds_read_b64_tr_b16 v[70:71], v179 offset:45056\n"
"ds_read_b64_tr_b16 v[72:73], v180 offset:4096\n"
"v_mfma_f32_16x16x32_bf16 v[232:235], v[40:43], v[160:163], v[232:235]\n"
"s_lshr_b32 s99, s6, 9\n"
"ds_read_b64_tr_b16 v[74:75], v180 offset:45056\n"
"ds_read_b64_tr_b16 v[76:77], v181 offset:4096\n"
"v_mfma_f32_16x16x32_bf16 v[236:239], v[44:47], v[160:163], v[236:239]\n"
"buffer_load_dwordx4 v192, s[32:35], s6 offen lds\n"
"ds_read_b64_tr_b16 v[78:79], v181 offset:45056\n"
"ds_read_b128 v[172:175], v183 offset:6144\n"
"s_waitcnt lgkmcnt(10)\n"
"v_mfma_f32_16x16x32_bf16 v[224:227], v[48:51], v[164:167], v[224:227]\n"
"buffer_load_dwordx4 v194, s[32:35], s6 offen offset:1024 lds\n"
"ds_read_b64_tr_b16 v[80:81], v188 offset:4096\n"
"ds_read_b64_tr_b16 v[82:83], v188 offset:45056\n"
"v_mfma_f32_16x16x32_bf16 v[228:231], v[52:55], v[164:167], v[228:231]\n"
"buffer_load_dwordx4 v196, s[24:27], s99 offen offset:2048 lds\n"
"ds_read_b64_tr_b16 v[84:85], v189 offset:4096\n"
"ds_read_b64_tr_b16 v[86:87], v189 offset:45056\n"
"v_mfma_f32_16x16x32_bf16 v[232:235], v[56:59], v[164:167], v[232:235]\n"
"buffer_load_dwordx4 v198, s[24:27], s99 offen offset:3072 lds\n"
"ds_read_b64_tr_b16 v[88:89], v190 offset:4096\n"
"ds_read_b64_tr_b16 v[90:91], v190 offset:45056\n"
"v_mfma_f32_16x16x32_bf16 v[236:239], v[60:63], v[164:167], v[236:239]\n"
"s_add_u32 s6, s6, 0x10000\n"
"ds_read_b64_tr_b16 v[92:93], v191 offset:4096\n"
"ds_read_b64_tr_b16 v[94:95], v191 offset:45056\n"
"s_waitcnt vmcnt(8)\n"
"s_barrier\n"
"ds_read_b128 v[160:163], v182 offset:10240\n"
"ds_read_b64_tr_b16 v[32:33], v178 offset:8192\n"
"s_waitcnt lgkmcnt(11)\n"
"v_mfma_f32_16x16x32_bf16 v[224:227], v[64:67], v[168:171], v[224:227]\n"
"s_and_b32 s6, s6, 0x7ffff\n"
"ds_read_b64_tr_b16 v[34:35], v178 offset:49152\n"
"ds_read_b64_tr_b16 v[36:37], v179 offset:8192\n"
"v_mfma_f32_16x16x32_bf16 v[228:231], v[68:71], v[168:171], v[228:231]\n"
"s_add_u32 m0, s64, 20480\n"
"ds_read_b64_tr_b16 v[38:39], v179 offset:49152\n"
"ds_read_b64_tr_b16 v[40:41], v180 offset:8192\n"
"v_mfma_f32_16x16x32_bf16 v[232:235], v[72:75], v[168:171], v[232:235]\n"
"s_lshr_b32 s99, s6, 9\n"
"ds_read_b64_tr_b16 v[42:43], v180 offset:49152\n"
"ds_read_b64_tr_b16 v[44:45], v181 offset:8192\n"
"v_mfma_f32_16x16x32_bf16 v[236:239], v[76:79], v[168:171], v[236:239]\n"
"buffer_load_dwordx4 v192, s[32:35], s6 offen lds\n"
"ds_read_b64_tr_b16 v[46:47], v181 offset:49152\n"
"ds_read_b128 v[164:167], v183 offset:10240\n"
"s_waitcnt lgkmcnt(10)\n"
"v_mfma_f32_16x16x32_bf16 v[224:227], v[80:83], v[172:175], v[224:227]\n"
"buffer_load_dwordx4 v194, s[32:35], s6 offen offset:1024 lds\n"
"ds_read_b64_tr_b16 v[48:49], v188 offset:8192\n"
"ds_read_b64_tr_b16 v[50:51], v188 offset:49152\n"
"v_mfma_f32_16x16x32_bf16 v[228:231], v[84:87], v[172:175], v[228:231]\n"
"buffer_load_dwordx4 v196, s[24:27], s99 offen offset:2048 lds\n"
"ds_read_b64_tr_b16 v[52:53], v189 offset:8192\n"
"ds_read_b64_tr_b16 v[54:55], v189 offset:49152\n"
"v_mfma_f32_16x16x32_bf16 v[232:235], v[88:91], v[172:175], v[232:235]\n"
"buffer_load_dwordx4 v198, s[24:27], s99 offen offset:3072 lds\n"
"ds_read_b64_tr_b16 v[56:57], v190 offset:8192\n"
"ds_read_b64_tr_b16 v[58:59], v190 offset:49152\n"
"v_mfma_f32_16x16x32_bf16 v[236:239], v[92:95], v[172:175], v[236:239]\n"
"s_add_u32 s6, s6, 0x10000\n"
"ds_read_b64_tr_b16 v[60:61], v191 offset:8192\n"
"ds_read_b64_tr_b16 v[62:63], v191 offset:49152\n"
"s_waitcnt vmcnt(8)\n"
"s_barrier\n"
"ds_read_b128 v[168:171], v182 offset:14336\n"
"ds_read_b64_tr_b16 v[64:65], v178 offset:12288\n"
"s_waitcnt lgkmcnt(11)\n"
"v_mfma_f32_16x16x32_bf16 v[224:227], v[32:35], v[160:163], v[224:227]\n"
"s_and_b32 s6, s6, 0x7ffff\n"
"ds_read_b64_tr_b16 v[66:67], v178 offset:53248\n"
"ds_read_b64_tr_b16 v[68:69], v179 offset:12288\n"
"v_mfma_f32_16x16x32_bf16 v[228:231], v[36:39], v[160:163], v[228:231]\n"
"s_add_u32 m0, s64, 24576\n"
"ds_read_b64_tr_b16 v[70:71], v179 offset:53248\n"
"ds_read_b64_tr_b16 v[72:73], v180 offset:12288\n"
"v_mfma_f32_16x16x32_bf16 v[232:235], v[40:43], v[160:163], v[232:235]\n"
"s_lshr_b32 s99, s6, 9\n"
"ds_read_b64_tr_b16 v[74:75], v180 offset:53248\n"
"ds_read_b64_tr_b16 v[76:77], v181 offset:12288\n"
"v_mfma_f32_16x16x32_bf16 v[236:239], v[44:47], v[160:163], v[236:239]\n"
"buffer_load_dwordx4 v192, s[32:35], s6 offen lds\n"
"ds_read_b64_tr_b16 v[78:79], v181 offset:53248\n"
"ds_read_b128 v[172:175], v183 offset:14336\n"
"s_waitcnt lgkmcnt(10)\n"
"v_mfma_f32_16x16x32_bf16 v[224:227], v[48:51], v[164:167], v[224:227]\n"
"buffer_load_dwordx4 v194, s[32:35], s6 offen offset:1024 lds\n"
"ds_read_b64_tr_b16 v[80:81], v188 offset:12288\n"
"ds_read_b64_tr_b16 v[82:83], v188 offset:53248\n"
"v_mfma_f32_16x16x32_bf16 v[228:231], v[52:55], v[164:167], v[228:231]\n"
"buffer_load_dwordx4 v196, s[24:27], s99 offen offset:2048 lds\n"
"ds_read_b64_tr_b16 v[84:85], v189 offset:12288\n"
"ds_read_b64_tr_b16 v[86:87], v189 offset:53248\n"
"v_mfma_f32_16x16x32_bf16 v[232:235], v[56:59], v[164:167], v[232:235]\n"
"buffer_load_dwordx4 v198, s[24:27], s99 offen offset:3072 lds\n"
"ds_read_b64_tr_b16 v[88:89], v190 offset:12288\n"
"ds_read_b64_tr_b16 v[90:91], v190 offset:53248\n"
"v_mfma_f32_16x16x32_bf16 v[236:239], v[60:63], v[164:167], v[236:239]\n"
"s_add_u32 s6, s6, 0x10000\n"
"ds_read_b64_tr_b16 v[92:93], v191 offset:12288\n"
"ds_read_b64_tr_b16 v[94:95], v191 offset:53248\n"
"v_add_u32_e32 v178, 16384, v178\n"
"v_add_u32_e32 v188, 16384, v188\n"
"v_add_u32_e32 v179, 16384, v179\n"
"v_add_u32_e32 v189, 16384, v189\n"
"v_add_u32_e32 v180, 16384, v180\n"
"v_add_u32_e32 v190, 16384, v190\n"
"v_add_u32_e32 v181, 16384, v181\n"
"v_add_u32_e32 v191, 16384, v191\n"
"s_waitcnt vmcnt(8)\n"
"s_barrier\n"
"ds_read_b128 v[160:163], v182 offset:18432\n"
"ds_read_b64_tr_b16 v[32:33], v178 offset:0\n"
"s_waitcnt lgkmcnt(11)\n"
"v_mfma_f32_16x16x32_bf16 v[224:227], v[64:67], v[168:171], v[224:227]\n"
"s_and_b32 s6, s6, 0x7ffff\n"
"s_add_u32 m0, s64, 28672\n"
"ds_read_b64_tr_b16 v[34:35], v178 offset:40960\n"
"ds_read_b64_tr_b16 v[36:37], v179 offset:0\n"
"v_mfma_f32_16x16x32_bf16 v[228:231], v[68:71], v[168:171], v[228:231]\n"
"s_lshr_b32 s99, s6, 9\n"
"buffer_load_dwordx4 v192, s[32:35], s6 offen lds\n"
"ds_read_b64_tr_b16 v[38:39], v179 offset:40960\n"
"ds_read_b64_tr_b16 v[40:41], v180 offset:0\n"
"v_mfma_f32_16x16x32_bf16 v[232:235], v[72:75], v[168:171], v[232:235]\n"
"buffer_load_dwordx4 v194, s[32:35], s6 offen offset:1024 lds\n"
"buffer_load_dwordx4 v196, s[24:27], s99 offen offset:2048 lds\n"
"ds_read_b64_tr_b16 v[42:43], v180 offset:40960\n"
"ds_read_b64_tr_b16 v[44:45], v181 offset:0\n"
"v_mfma_f32_16x16x32_bf16 v[236:239], v[76:79], v[168:171], v[236:239]\n"
"buffer_load_dwordx4 v198, s[24:27], s99 offen offset:3072 lds\n"
"ds_read_b64_tr_b16 v[46:47], v181 offset:40960\n"
"ds_read_b128 v[164:167], v183 offset:18432\n"
"s_waitcnt lgkmcnt(10)\n"
"v_mfma_f32_16x16x32_bf16 v[224:227], v[80:83], v[172:175], v[224:227]\n"
"s_add_u32 s6, s6, 0x10000\n"
"ds_read_b64_tr_b16 v[48:49], v188 offset:0\n"
"ds_read_b64_tr_b16 v[50:51], v188 offset:40960\n"
"v_mfma_f32_16x16x32_bf16 v[228:231], v[84:87], v[172:175], v[228:231]\n"
"v_mov_b32_e32 v140, -1\n"
"ds_read_b64_tr_b16 v[52:53], v189 offset:0\n"
"ds_read_b64_tr_b16 v[54:55], v189 offset:40960\n"
"v_mfma_f32_16x16x32_bf16 v[232:235], v[88:91], v[172:175], v[232:235]\n"
"v_mov_b32_e32 v141, -1\n"
"ds_read_b64_tr_b16 v[56:57], v190 offset:0\n"
"ds_read_b64_tr_b16 v[58:59], v190 offset:40960\n"
"v_mfma_f32_16x16x32_bf16 v[236:239], v[92:95], v[172:175], v[236:239]\n"
"v_mov_b32_e32 v142, -1\n"
"ds_read_b64_tr_b16 v[60:61], v191 offset:0\n"
"ds_read_b64_tr_b16 v[62:63], v191 offset:40960\n"
"v_mov_b32_e32 v143, -1\n"
"s_add_u32 s100, s81, 2097152\n"
"buffer_store_dwordx4 v[140:143], v1, s[52:55], s100 offen sc1\n"
"buffer_store_dwordx4 v[140:143], v1, s[52:55], s100 offen offset:1024 sc1\n"
"s_waitcnt vmcnt(10)\n"
"s_barrier\n"
"ds_read_b128 v[168:171], v182 offset:22528\n"
"ds_read_b64_tr_b16 v[64:65], v178 offset:4096\n"
"s_waitcnt lgkmcnt(11)\n"
"v_mfma_f32_16x16x32_bf16 v[224:227], v[32:35], v[160:163], v[224:227]\n"
"s_mul_i32 s99, s77, 1572864\n"
"s_add_u32 s99, s99, s101\n"
"ds_read_b64_tr_b16 v[66:67], v178 offset:45056\n"
"ds_read_b64_tr_b16 v[68:69], v179 offset:4096\n"
"v_mfma_f32_16x16x32_bf16 v[228:231], v[36:39], v[160:163], v[228:231]\n"
"s_add_u32 s99, s99, 524288\n"
"buffer_load_dwordx4 v[96:99], v21, s[36:39], s99 offen\n"
"ds_read_b64_tr_b16 v[70:71], v179 offset:45056\n"
"ds_read_b64_tr_b16 v[72:73], v180 offset:4096\n"
"v_mfma_f32_16x16x32_bf16 v[232:235], v[40:43], v[160:163], v[232:235]\n"
"s_mul_i32 s99, s77, 4291297280\n"
"s_add_u32 s99, s99, s101\n"
"ds_read_b64_tr_b16 v[74:75], v180 offset:45056\n"
"ds_read_b64_tr_b16 v[76:77], v181 offset:4096\n"
"v_mfma_f32_16x16x32_bf16 v[236:239], v[44:47], v[160:163], v[236:239]\n"
"s_add_u32 s99, s99, 3670016\n"
"buffer_load_dwordx4 v[100:103], v21, s[36:39], s99 offen\n"
"ds_read_b64_tr_b16 v[78:79], v181 offset:45056\n"
"ds_read_b128 v[172:175], v183 offset:22528\n"
"s_waitcnt lgkmcnt(10)\n"
"v_mfma_f32_16x16x32_bf16 v[224:227], v[48:51], v[164:167], v[224:227]\n"
"s_mul_i32 s99, s77, 1572864\n"
"s_add_u32 s99, s99, s101\n"
"ds_read_b64_tr_b16 v[80:81], v188 offset:4096\n"
"ds_read_b64_tr_b16 v[82:83], v188 offset:45056\n"
"v_mfma_f32_16x16x32_bf16 v[228:231], v[52:55], v[164:167], v[228:231]\n"
"s_add_u32 s99, s99, 1572864\n"
"buffer_load_dwordx4 v[104:107], v21, s[36:39], s99 offen\n"
"ds_read_b64_tr_b16 v[84:85], v189 offset:4096\n"
"ds_read_b64_tr_b16 v[86:87], v189 offset:45056\n"
"v_mfma_f32_16x16x32_bf16 v[232:235], v[56:59], v[164:167], v[232:235]\n"
"s_mul_i32 s99, s77, 1572864\n"
"s_add_u32 s99, s99, s101\n"
"ds_read_b64_tr_b16 v[88:89], v190 offset:4096\n"
"ds_read_b64_tr_b16 v[90:91], v190 offset:45056\n"
"v_mfma_f32_16x16x32_bf16 v[236:239], v[60:63], v[164:167], v[236:239]\n"
"s_add_u32 s99, s99, 1048576\n"
"buffer_load_dwordx4 v[108:111], v21, s[36:39], s99 offen\n"
"ds_read_b64_tr_b16 v[92:93], v191 offset:4096\n"
"ds_read_b64_tr_b16 v[94:95], v191 offset:45056\n"
"s_mul_i32 s99, s77, 0\n"
"s_add_u32 s99, s99, s101\n"
"s_add_u32 s99, s99, 4194304\n"
"buffer_load_dwordx4 v[112:115], v21, s[36:39], s99 offen\n"
"s_mov_b32 s40, s16\n"
"s_and_b32 s41, s17, 0xffff\n"
"s_mov_b32 s42, 524288\n"
"s_mov_b32 s43, 0x20000\n"
"s_lshr_b32 s99, s81, 2\n"
"s_and_b32 s99, s99, 0x7fc00\n"
"buffer_load_dwordx4 v[116:119], v1, s[40:43], s99 offen\n"
"s_waitcnt vmcnt(12)\n"
"s_barrier\n"
"ds_read_b128 v[160:163], v182 offset:26624\n"
"ds_read_b64_tr_b16 v[32:33], v178 offset:8192\n"
"s_waitcnt lgkmcnt(11)\n"
"v_mfma_f32_16x16x32_bf16 v[224:227], v[64:67], v[168:171], v[224:227]\n"
"ds_read_b64_tr_b16 v[34:35], v178 offset:49152\n"
"ds_read_b64_tr_b16 v[36:37], v179 offset:8192\n"
"v_mfma_f32_16x16x32_bf16 v[228:231], v[68:71], v[168:171], v[228:231]\n"
"ds_read_b64_tr_b16 v[38:39], v179 offset:49152\n"
"ds_read_b64_tr_b16 v[40:41], v180 offset:8192\n"
"v_mfma_f32_16x16x32_bf16 v[232:235], v[72:75], v[168:171], v[232:235]\n"
"ds_read_b64_tr_b16 v[42:43], v180 offset:49152\n"
"ds_read_b64_tr_b16 v[44:45], v181 offset:8192\n"
"v_mfma_f32_16x16x32_bf16 v[236:239], v[76:79], v[168:171], v[236:239]\n"
"ds_read_b64_tr_b16 v[46:47], v181 offset:49152\n"
"ds_read_b128 v[164:167], v183 offset:26624\n"
"s_waitcnt lgkmcnt(10)\n"
"v_mfma_f32_16x16x32_bf16 v[224:227], v[80:83], v[172:175], v[224:227]\n"
"ds_read_b64_tr_b16 v[48:49], v188 offset:8192\n"
"ds_read_b64_tr_b16 v[50:51], v188 offset:49152\n"
"v_mfma_f32_16x16x32_bf16 v[228:231], v[84:87], v[172:175], v[228:231]\n"
"ds_read_b64_tr_b16 v[52:53], v189 offset:8192\n"
"ds_read_b64_tr_b16 v[54:55], v189 offset:49152\n"
"v_mfma_f32_16x16x32_bf16 v[232:235], v[88:91], v[172:175], v[232:235]\n"
"ds_read_b64_tr_b16 v[56:57], v190 offset:8192\n"
"ds_read_b64_tr_b16 v[58:59], v190 offset:49152\n"
"v_mfma_f32_16x16x32_bf16 v[236:239], v[92:95], v[172:175], v[236:239]\n"
"ds_read_b64_tr_b16 v[60:61], v191 offset:8192\n"
"ds_read_b64_tr_b16 v[62:63], v191 offset:49152\n"
"s_waitcnt vmcnt(8)\n"
"s_barrier\n"
"ds_read_b128 v[168:171], v182 offset:30720\n"
"ds_read_b64_tr_b16 v[64:65], v178 offset:12288\n"
"s_waitcnt lgkmcnt(11)\n"
"v_mfma_f32_16x16x32_bf16 v[224:227], v[32:35], v[160:163], v[224:227]\n"
"ds_read_b64_tr_b16 v[66:67], v178 offset:53248\n"
"ds_read_b64_tr_b16 v[68:69], v179 offset:12288\n"
"v_mfma_f32_16x16x32_bf16 v[228:231], v[36:39], v[160:163], v[228:231]\n"
"ds_read_b64_tr_b16 v[70:71], v179 offset:53248\n"
"ds_read_b64_tr_b16 v[72:73], v180 offset:12288\n"
"v_mfma_f32_16x16x32_bf16 v[232:235], v[40:43], v[160:163], v[232:235]\n"
"ds_read_b64_tr_b16 v[74:75], v180 offset:53248\n"
"ds_read_b64_tr_b16 v[76:77], v181 offset:12288\n"
"v_mfma_f32_16x16x32_bf16 v[236:239], v[44:47], v[160:163], v[236:239]\n"
"ds_read_b64_tr_b16 v[78:79], v181 offset:53248\n"
"ds_read_b128 v[172:175], v183 offset:30720\n"
"s_waitcnt lgkmcnt(10)\n"
"v_mfma_f32_16x16x32_bf16 v[224:227], v[48:51], v[164:167], v[224:227]\n"
"ds_read_b64_tr_b16 v[80:81], v188 offset:12288\n"
"ds_read_b64_tr_b16 v[82:83], v188 offset:53248\n"
"v_mfma_f32_16x16x32_bf16 v[228:231], v[52:55], v[164:167], v[228:231]\n"
"ds_read_b64_tr_b16 v[84:85], v189 offset:12288\n"
"ds_read_b64_tr_b16 v[86:87], v189 offset:53248\n"
"v_mfma_f32_16x16x32_bf16 v[232:235], v[56:59], v[164:167], v[232:235]\n"
"ds_read_b64_tr_b16 v[88:89], v190 offset:12288\n"
"ds_read_b64_tr_b16 v[90:91], v190 offset:53248\n"
"v_mfma_f32_16x16x32_bf16 v[236:239], v[60:63], v[164:167], v[236:239]\n"
"ds_read_b64_tr_b16 v[92:93], v191 offset:12288\n"
"ds_read_b64_tr_b16 v[94:95], v191 offset:53248\n"
"s_waitcnt lgkmcnt(15)\n"
"v_mfma_f32_16x16x32_bf16 v[224:227], v[64:67], v[168:171], v[224:227]\n"
"s_waitcnt lgkmcnt(13)\n"
"v_mfma_f32_16x16x32_bf16 v[228:231], v[68:71], v[168:171], v[228:231]\n"
"s_waitcnt lgkmcnt(11)\n"
"v_mfma_f32_16x16x32_bf16 v[232:235], v[72:75], v[168:171], v[232:235]\n"
"s_waitcnt lgkmcnt(9)\n"
"v_mfma_f32_16x16x32_bf16 v[236:239], v[76:79], v[168:171], v[236:239]\n"
"s_waitcnt lgkmcnt(6)\n"
"v_mfma_f32_16x16x32_bf16 v[224:227], v[80:83], v[172:175], v[224:227]\n"
"s_waitcnt lgkmcnt(4)\n"
"v_mfma_f32_16x16x32_bf16 v[228:231], v[84:87], v[172:175], v[228:231]\n"
"s_waitcnt lgkmcnt(2)\n"
"v_mfma_f32_16x16x32_bf16 v[232:235], v[88:91], v[172:175], v[232:235]\n"
"s_waitcnt lgkmcnt(0)\n"
"v_mfma_f32_16x16x32_bf16 v[236:239], v[92:95], v[172:175], v[236:239]\n"
"s_lshr_b32 s6, s81, 3\n"
"s_add_u32 s6, s6, 1048576\n"
"s_andn2_b32 s7, s6, 2047\n"
"v_pk_add_f32 v[32:33], v[224:225], v[226:227]\n"
"v_pk_add_f32 v[34:35], v[228:229], v[230:231]\n"
"s_nop 0\n"
"v_pk_add_f32 v[36:37], v[232:233], v[234:235]\n"
"s_nop 0\n"
"v_pk_add_f32 v[38:39], v[236:237], v[238:239]\n"
"v_pk_add_f32 v[32:33], v[32:33], v[34:35]\n"
"v_pk_add_f32 v[36:37], v[36:37], v[38:39]\n"
"v_pk_add_f32 v[32:33], v[32:33], v[36:37]\n"
"v_add_f32_e32 v248, v32, v33\n"
"v_mov_b32_e32 v32, v248\n"
"s_nop 1\n"
"v_permlane16_swap_b32 v248, v32\n"
"v_add_f32_e32 v248, v248, v32\n"
"v_mov_b32_e32 v32, v248\n"
"s_nop 1\n"
"v_permlane32_swap_b32 v248, v32\n"
"v_add_f32_e32 v248, v248, v32\n"
"v_mul_f32_e32 v248, 0.015625, v248\n"
"v_pk_add_f32 v[32:33], v[224:225], v[248:249] op_sel_hi:[1,0] neg_lo:[0,1] neg_hi:[0,1]\n"
"v_pk_add_f32 v[34:35], v[226:227], v[248:249] op_sel_hi:[1,0] neg_lo:[0,1] neg_hi:[0,1]\n"
"v_pk_add_f32 v[36:37], v[228:229], v[248:249] op_sel_hi:[1,0] neg_lo:[0,1] neg_hi:[0,1]\n"
"v_pk_add_f32 v[38:39], v[230:231], v[248:249] op_sel_hi:[1,0] neg_lo:[0,1] neg_hi:[0,1]\n"
"v_pk_add_f32 v[40:41], v[232:233], v[248:249] op_sel_hi:[1,0] neg_lo:[0,1] neg_hi:[0,1]\n"
"v_pk_add_f32 v[42:43], v[234:235], v[248:249] op_sel_hi:[1,0] neg_lo:[0,1] neg_hi:[0,1]\n"
"v_pk_add_f32 v[44:45], v[236:237], v[248:249] op_sel_hi:[1,0] neg_lo:[0,1] neg_hi:[0,1]\n"
"v_pk_add_f32 v[46:47], v[238:239], v[248:249] op_sel_hi:[1,0] neg_lo:[0,1] neg_hi:[0,1]\n"
"v_pk_mul_f32 v[48:49], v[32:33], v[32:33]\n"
"v_pk_mul_f32 v[50:51], v[34:35], v[34:35]\n"
"v_pk_fma_f32 v[48:49], v[36:37], v[36:37], v[48:49]\n"
"v_pk_fma_f32 v[50:51], v[38:39], v[38:39], v[50:51]\n"
"v_pk_fma_f32 v[48:49], v[40:41], v[40:41], v[48:49]\n"
"v_pk_fma_f32 v[50:51], v[42:43], v[42:43], v[50:51]\n"
"v_pk_fma_f32 v[48:49], v[44:45], v[44:45], v[48:49]\n"
"v_pk_fma_f32 v[50:51], v[46:47], v[46:47], v[50:51]\n"
"v_pk_add_f32 v[48:49], v[48:49], v[50:51]\n"
"v_add_f32_e32 v250, v48, v49\n"
"v_mov_b32_e32 v32, v250\n"
"s_nop 1\n"
"v_permlane16_swap_b32 v250, v32\n"
"v_add_f32_e32 v250, v250, v32\n"
"v_mov_b32_e32 v32, v250\n"
"s_nop 1\n"
"v_permlane32_swap_b32 v250, v32\n"
"v_add_f32_e32 v250, v250, v32\n"
"s_waitcnt vmcnt(6)\n"
"buffer_store_dwordx4 v[248:251], v25, s[52:55], s6 offen sc1\n"
"s_barrier\n"
"s_lshl_b32 s99, s56, 10\n"
"v_add_u32_e32 v186, s99, v1\n"
"ds_write_b128 v186, v[224:227] offset:0\n"
"ds_write_b128 v186, v[228:231] offset:4096\n"
"ds_write_b128 v186, v[232:235] offset:8192\n"
"ds_write_b128 v186, v[236:239] offset:12288\n"
"s_waitcnt lgkmcnt(0)\n"
"s_add_u32 m0, s13, 32768\n"
"s_add_u32 s100, s78, 524288\n"
"buffer_load_dwordx4 v3, s[36:39], s100 offen offset:0 lds\n"
"buffer_load_dwordx4 v4, s[36:39], s100 offen offset:1024 lds\n"
"s_lshl2_add_u32 s88, s57, s56\n"
"s_lshl_b32 s88, s88, 10\n"
"s_lshl_b32 s87, s60, 18\n"
"s_add_u32 s88, s88, s87\n"
"s_sub_u32 s88, s88, 32768\n"
"s_add_u32 s86, s57, 1\n"
"s_and_b32 s86, s86, 3\n"
"s_lshl_b32 s87, s86, 16\n"
"s_add_u32 s24, s87, s88\n"
"s_add_u32 s25, s24, 65536\n"
"s_cmp_ge_u32 s86, 3\n"
"s_cselect_b32 s87, -3, 2\n"
"s_lshl_b32 s87, s87, 16\n"
"s_add_u32 s26, s24, s87\n"
"s_cmp_ge_u32 s86, 2\n"
"s_cselect_b32 s87, -2, 3\n"
"buffer_load_dwordx4 v5, s[36:39], s100 offen offset:2048 lds\n"
"s_lshl_b32 s87, s87, 16\n"
"s_add_u32 s27, s24, s87\n"
"s_cmp_ge_u32 s86, 1\n"
"s_cselect_b32 s87, -1, 4\n"
"s_lshl_b32 s87, s87, 16\n"
"s_add_u32 s32, s24, s87\n"
"s_cmp_gt_u32 s86, 2\n"
"s_cselect_b32 s87, 2, 1\n"
"s_cselect_b32 s85, 3, 2\n"
"s_lshl_b32 s33, s87, 14\n"
"s_lshl_b32 s72, s85, 14\n"
"s_cmp_gt_u32 s86, 1\n"
"s_cselect_b32 s87, 3, 2\n"
"s_cselect_b32 s85, 4, 3\n"
"s_lshl_b32 s34, s87, 14\n"
"s_lshl_b32 s73, s85, 14\n"
"buffer_load_dwordx4 v6, s[36:39], s100 offen offset:3072 lds\n"
"s_cmp_gt_u32 s86, 0\n"
"s_cselect_b32 s87, 4, 3\n"
"s_cselect_b32 s85, 0, 4\n"
"s_lshl_b32 s35, s87, 14\n"
"s_lshl_b32 s74, s85, 14\n"
"s_mov_b32 s40, s16\n"
"s_and_b32 s41, s17, 0xffff\n"
"s_mov_b32 s42, 524288\n"
"s_mov_b32 s43, 0x20000\n"
"s_mov_b32 s44, s8\n"
"s_and_b32 s45, s9, 0xffff\n"
"s_mov_b32 s46, 2097152\n"
"s_mov_b32 s47, 0x20000\n"
"s_mov_b32 s48, s20\n"
"s_and_b32 s49, s21, 0xffff\n"
"s_mov_b32 s50, 2097152\n"
"s_add_u32 m0, s13, 49152\n"
"s_add_u32 s100, s78, 2097152\n"
"buffer_load_dwordx4 v3, s[36:39], s100 offen offset:0 lds\n"
"s_mov_b32 s51, 0x20000\n"
"v_add_u32_e32 v15, 2048, v13\n"
"v_add_u32_e32 v16, 17408, v13\n"
"s_and_b32 s86, s56, 1\n"
"s_lshr_b32 s88, s56, 1\n"
"s_lshl_b32 s88, s88, 6\n"
"v_xor_b32_e32 v9, s88, v9\n"
"v_add_u32_e32 v9, 81920, v9\n"
"v_add_u32_e32 v12, 49152, v9\n"
"s_lshl_b32 s84, s62, 7\n"
"s_lshl_b32 s83, s56, 15\n"
"s_add_u32 s12, s83, s84\n"
"s_lshl_b32 s83, s63, 16\n"
"s_lshl_b32 s87, s86, 10\n"
"s_add_u32 s14, s83, s87\n"
"s_add_u32 s14, s14, s84\n"
"buffer_load_dwordx4 v4, s[36:39], s100 offen offset:1024 lds\n"
"s_add_u32 s14, s14, s88\n"
"s_add_u32 s80, s14, 32768\n"
"s_add_u32 s83, s83, s13\n"
"s_add_u32 s17, s83, 4194304\n"
"s_lshl_b32 s87, s86, 14\n"
"s_add_u32 s75, s87, 81920\n"
"v_add_u32_e32 v29, s75, v1\n"
"v_add_u32_e32 v30, 49152, v11\n"
"s_sub_u32 s18, s17, s13\n"
"s_add_u32 s18, s18, s87\n"
"s_lshl_b32 s83, s63, 16\n"
"s_lshl2_add_u32 s87, s62, s56\n"
"s_lshl_b32 s87, s87, 9\n"
"s_add_u32 s83, s83, s87\n"
"s_add_u32 s15, s83, 4194304\n"
"s_add_u32 s16, s15, 32768\n"
"buffer_load_dwordx4 v5, s[36:39], s100 offen offset:2048 lds\n"
"s_add_u32 s19, s81, 4194304\n"
"s_add_u32 s20, s6, 262144\n"
"s_mov_b32 s79, 262144\n"
"s_lshl_b32 s88, s58, 5\n"
"s_add_u32 s88, s88, s62\n"
"s_lshl2_add_u32 s88, s88, s56\n"
"s_lshl_b32 s88, s88, 11\n"
"s_add_u32 s87, s57, 1\n"
"s_and_b32 s87, s87, 3\n"
"s_lshl_b32 s87, s87, 16\n"
"s_add_u32 s87, s87, s88\n"
"s_add_u32 s8, s87, 6291456\n"
"s_add_u32 s87, s57, 2\n"
"s_and_b32 s87, s87, 3\n"
"s_lshl_b32 s87, s87, 16\n"
"s_add_u32 s87, s87, s88\n"
"buffer_load_dwordx4 v6, s[36:39], s100 offen offset:3072 lds\n"
"s_add_u32 s9, s87, 8388608\n"
"s_add_u32 s87, s57, 3\n"
"s_and_b32 s87, s87, 3\n"
"s_lshl_b32 s87, s87, 16\n"
"s_add_u32 s87, s87, s88\n"
"s_add_u32 s10, s87, 10485760\n"
"s_add_u32 s87, s57, 4\n"
"s_and_b32 s87, s87, 3\n"
"s_lshl_b32 s87, s87, 16\n"
"s_add_u32 s87, s87, s88\n"
"s_add_u32 s11, s87, 6291456\n"
"v_and_b32_e32 v244, 1, v242\n"
"v_lshlrev_b32_e32 v244, 14, v244\n"
"v_lshrrev_b32_e32 v246, 1, v242\n"
"v_lshl_add_u32 v244, v246, 8, v244\n"
"v_lshl_add_u32 v23, v243, 4, v244\n"
"s_add_u32 m0, s13, 65536\n"
"s_add_u32 s100, s78, 3670016\n"
"buffer_load_dwordx4 v3, s[36:39], s100 offen offset:0 lds\n"
"v_lshlrev_b32_e32 v244, 4, v242\n"
"v_lshl_add_u32 v24, v243, 11, v244\n"
"s_or_b32 s87, s62, s57\n"
"s_or_b32 s87, s87, s56\n"
"s_lshl3_add_u32 s84, s77, s60\n"
"s_lshl2_add_u32 s84, s84, 16\n"
"s_cmp_eq_u32 s87, 0\n"
"s_cselect_b32 s84, s84, 2130706432\n"
"v_max_u32_e32 v26, s84, v28\n"
"v_bfe_u32 v244, v241, 2, 2\n"
"v_and_b32_e32 v245, 1, v241\n"
"v_lshlrev_b32_e32 v245, 4, v245\n"
"v_mov_b32_e32 v246, 0x3f80\n"
"v_lshlrev_b32_e32 v246, v245, v246\n"
"v_cmp_eq_u32_e32 vcc, v244, v242\n"
"s_nop 1\n"
"v_cndmask_b32_e32 v246, 0, v246, vcc\n"
"buffer_load_dwordx4 v4, s[36:39], s100 offen offset:1024 lds\n"
"v_and_b32_e32 v245, 2, v241\n"
"v_cmp_eq_u32_e32 vcc, 0, v245\n"
"s_nop 1\n"
"v_cndmask_b32_e32 v202, 0, v246, vcc\n"
"v_xor_b32_e32 v203, v202, v246\n"
"v_mov_b32_e32 v200, 0\n"
"v_mov_b32_e32 v201, 0\n"
"v_mov_b32_e32 v204, 0\n"
"v_mov_b32_e32 v205, 0\n"
"buffer_load_dwordx4 v[64:67], v1, s[52:55], s7 offen sc1\n"
"buffer_load_dwordx4 v[68:71], v1, s[52:55], s7 offen offset:1024 sc1\n"
"buffer_load_dwordx4 v5, s[36:39], s100 offen offset:2048 lds\n"
"buffer_load_dwordx4 v6, s[36:39], s100 offen offset:3072 lds\n"
"s_add_u32 m0, s13, 131072\n"
"s_add_u32 s100, s78, 0\n"
"buffer_load_dwordx4 v3, s[36:39], s100 offen offset:0 lds\n"
"buffer_load_dwordx4 v4, s[36:39], s100 offen offset:1024 lds\n"
"s_waitcnt vmcnt(4)\n"
".Lcn_L_stchk:\n"
"v_xor_b32_e32 v32, s66, v65\n"
"v_bitop3_b32 v32, v67, s66, v32 bitop3:0xbe\n"
"v_bitop3_b32 v32, v69, s66, v32 bitop3:0xbe\n"
"v_bitop3_b32 v32, v71, s66, v32 bitop3:0xbe\n"
"v_cmp_ne_u32_e32 vcc, 0, v32\n"
"v_add_f32_e32 v34, v64, v68\n"
"s_cbranch_vccnz .Lcn_R_st\n"
".Lcn_L_stok:\n"
"buffer_load_dword v72, v17, s[52:55], 0 offen sc1\n"
"v_mov_b32_e32 v32, v34\n"
"s_nop 1\n"
"v_permlane16_swap_b32 v34, v32\n"
"v_add_f32_e32 v34, v34, v32\n"
"v_mov_b32_e32 v32, v34\n"
"s_nop 1\n"
"v_permlane32_swap_b32 v34, v32\n"
"v_add_f32_e32 v34, v34, v32\n"
"v_mul_f32_e32 v34, 0.125, v34\n"
"v_sub_f32_e32 v36, v64, v34\n"
"v_sub_f32_e32 v37, v68, v34\n"
"v_add_f32_e32 v38, v66, v70\n"
"v_mul_f32_e32 v36, v36, v36\n"
"v_fmac_f32_e32 v36, v37, v37\n"
"v_fmac_f32_e32 v38, 0x42800000, v36\n"
"v_mov_b32_e32 v39, 0x3727c5ac\n"
"buffer_load_dwordx4 v5, s[36:39], s100 offen offset:2048 lds\n"
"v_mov_b32_e32 v32, v38\n"
"s_nop 1\n"
"v_permlane16_swap_b32 v38, v32\n"
"v_add_f32_e32 v38, v38, v32\n"
"v_mov_b32_e32 v32, v38\n"
"s_nop 1\n"
"v_permlane32_swap_b32 v38, v32\n"
"v_add_f32_e32 v38, v38, v32\n"
"v_fmac_f32_e32 v39, 0x3b000000, v38\n"
"v_rsq_f32_e32 v40, v39\n"
"s_add_u32 s87, s81, 2097152\n"
"v_mul_f32_e64 v41, -v34, v40\n"
"v_mul_f32_e32 v42, 0xbfb8aa3b, v40\n"
"v_mul_f32_e32 v43, 0xbfb8aa3b, v41\n"
"v_pk_fma_f32 v[160:161], v[224:225], v[42:43], v[42:43] op_sel:[0,0,1] op_sel_hi:[1,0,1]\n"
"v_pk_fma_f32 v[162:163], v[226:227], v[42:43], v[42:43] op_sel:[0,0,1] op_sel_hi:[1,0,1]\n"
"buffer_load_dwordx4 v6, s[36:39], s100 offen offset:3072 lds\n"
"v_pk_fma_f32 v[164:165], v[228:229], v[42:43], v[42:43] op_sel:[0,0,1] op_sel_hi:[1,0,1]\n"
"v_pk_fma_f32 v[166:167], v[230:231], v[42:43], v[42:43] op_sel:[0,0,1] op_sel_hi:[1,0,1]\n"
"v_exp_f32_e32 v160, v160\n"
"v_exp_f32_e32 v161, v161\n"
"v_pk_fma_f32 v[144:145], v[224:225], v[40:41], v[40:41] op_sel:[0,0,1] op_sel_hi:[1,0,1]\n"
"v_exp_f32_e32 v162, v162\n"
"v_exp_f32_e32 v163, v163\n"
"v_pk_fma_f32 v[146:147], v[226:227], v[40:41], v[40:41] op_sel:[0,0,1] op_sel_hi:[1,0,1]\n"
"v_exp_f32_e32 v164, v164\n"
"v_exp_f32_e32 v165, v165\n"
"v_pk_fma_f32 v[148:149], v[228:229], v[40:41], v[40:41] op_sel:[0,0,1] op_sel_hi:[1,0,1]\n"
"s_add_u32 m0, s13, 147456\n"
"s_add_u32 s100, s78, 1572864\n"
"buffer_load_dwordx4 v3, s[36:39], s100 offen offset:0 lds\n"
"v_exp_f32_e32 v166, v166\n"
"v_exp_f32_e32 v167, v167\n"
"v_pk_fma_f32 v[150:151], v[230:231], v[40:41], v[40:41] op_sel:[0,0,1] op_sel_hi:[1,0,1]\n"
"v_pk_add_f32 v[160:161], v[160:161], 1.0 op_sel_hi:[1,0]\n"
"v_pk_add_f32 v[162:163], v[162:163], 1.0 op_sel_hi:[1,0]\n"
"v_pk_add_f32 v[164:165], v[164:165], 1.0 op_sel_hi:[1,0]\n"
"v_pk_add_f32 v[166:167], v[166:167], 1.0 op_sel_hi:[1,0]\n"
"v_rcp_f32_e32 v160, v160\n"
"v_rcp_f32_e32 v161, v161\n"
"v_rcp_f32_e32 v162, v162\n"
"v_rcp_f32_e32 v163, v163\n"
"buffer_load_dwordx4 v4, s[36:39], s100 offen offset:1024 lds\n"
"v_rcp_f32_e32 v164, v164\n"
"v_rcp_f32_e32 v165, v165\n"
"v_rcp_f32_e32 v166, v166\n"
"v_rcp_f32_e32 v167, v167\n"
"v_pk_mul_f32 v[144:145], v[144:145], v[160:161]\n"
"v_pk_mul_f32 v[146:147], v[146:147], v[162:163]\n"
"v_pk_mul_f32 v[148:149], v[148:149], v[164:165]\n"
"v_pk_mul_f32 v[150:151], v[150:151], v[166:167]\n"
"v_cvt_pk_bf16_f32 v176, v144, v145\n"
"v_cvt_pk_bf16_f32 v177, v146, v147\n"
"v_cvt_pk_bf16_f32 v178, v148, v149\n"
"v_cvt_pk_bf16_f32 v179, v150, v151\n"
"buffer_load_dwordx4 v5, s[36:39], s100 offen offset:2048 lds\n"
"v_min_u32_e32 v176, -2, v176\n"
"v_min_u32_e32 v177, -2, v177\n"
"v_min_u32_e32 v178, -2, v178\n"
"v_min_u32_e32 v179, -2, v179\n"
"buffer_store_dwordx4 v[176:179], v1, s[52:55], s87 offen offset:0 sc1\n"
"v_pk_fma_f32 v[168:169], v[232:233], v[42:43], v[42:43] op_sel:[0,0,1] op_sel_hi:[1,0,1]\n"
"v_pk_fma_f32 v[170:171], v[234:235], v[42:43], v[42:43] op_sel:[0,0,1] op_sel_hi:[1,0,1]\n"
"v_pk_fma_f32 v[172:173], v[236:237], v[42:43], v[42:43] op_sel:[0,0,1] op_sel_hi:[1,0,1]\n"
"v_pk_fma_f32 v[174:175], v[238:239], v[42:43], v[42:43] op_sel:[0,0,1] op_sel_hi:[1,0,1]\n"
"v_exp_f32_e32 v168, v168\n"
"v_exp_f32_e32 v169, v169\n"
"v_pk_fma_f32 v[152:153], v[232:233], v[40:41], v[40:41] op_sel:[0,0,1] op_sel_hi:[1,0,1]\n"
"v_exp_f32_e32 v170, v170\n"
"buffer_load_dwordx4 v6, s[36:39], s100 offen offset:3072 lds\n"
"v_exp_f32_e32 v171, v171\n"
"v_pk_fma_f32 v[154:155], v[234:235], v[40:41], v[40:41] op_sel:[0,0,1] op_sel_hi:[1,0,1]\n"
"v_exp_f32_e32 v172, v172\n"
"v_exp_f32_e32 v173, v173\n"
"v_pk_fma_f32 v[156:157], v[236:237], v[40:41], v[40:41] op_sel:[0,0,1] op_sel_hi:[1,0,1]\n"
"v_exp_f32_e32 v174, v174\n"
"v_exp_f32_e32 v175, v175\n"
"v_pk_fma_f32 v[158:159], v[238:239], v[40:41], v[40:41] op_sel:[0,0,1] op_sel_hi:[1,0,1]\n"
"v_pk_add_f32 v[168:169], v[168:169], 1.0 op_sel_hi:[1,0]\n"
"v_pk_add_f32 v[170:171], v[170:171], 1.0 op_sel_hi:[1,0]\n"
"v_pk_add_f32 v[172:173], v[172:173], 1.0 op_sel_hi:[1,0]\n"
"s_add_u32 m0, s13, 81920\n"
"s_add_u32 s100, s78, 3145728\n"
"buffer_load_dwordx4 v3, s[36:39], s100 offen offset:0 lds\n"
"v_pk_add_f32 v[174:175], v[174:175], 1.0 op_sel_hi:[1,0]\n"
"v_rcp_f32_e32 v168, v168\n"
"v_rcp_f32_e32 v169, v169\n"
"v_rcp_f32_e32 v170, v170\n"
"v_rcp_f32_e32 v171, v171\n"
"v_rcp_f32_e32 v172, v172\n"
"v_rcp_f32_e32 v173, v173\n"
"v_rcp_f32_e32 v174, v174\n"
"v_rcp_f32_e32 v175, v175\n"
"v_pk_mul_f32 v[152:153], v[152:153], v[168:169]\n"
"buffer_load_dwordx4 v4, s[36:39], s100 offen offset:1024 lds\n"
"v_pk_mul_f32 v[154:155], v[154:155], v[170:171]\n"
"v_pk_mul_f32 v[156:157], v[156:157], v[172:173]\n"
"v_pk_mul_f32 v[158:159], v[158:159], v[174:175]\n"
"v_cvt_pk_bf16_f32 v180, v152, v153\n"
"v_cvt_pk_bf16_f32 v181, v154, v155\n"
"v_cvt_pk_bf16_f32 v182, v156, v157\n"
"v_cvt_pk_bf16_f32 v183, v158, v159\n"
"v_min_u32_e32 v180, -2, v180\n"
"v_min_u32_e32 v181, -2, v181\n"
"v_min_u32_e32 v182, -2, v182\n"
"v_min_u32_e32 v183, -2, v183\n"
"buffer_store_dwordx4 v[180:183], v1, s[52:55], s87 offen offset:1024 sc1\n"
"v_add_u32_e32 v224, 0, v1\n"
"v_add_u32_e32 v225, 0, v19\n"
"v_add_u32_e32 v226, 0, v20\n"
"v_add_u32_e32 v227, 16384, v1\n"
"v_add_u32_e32 v228, 16384, v19\n"
"v_add_u32_e32 v229, 16384, v20\n"
"s_waitcnt vmcnt(13)\n"
"s_barrier\n"
"buffer_load_dwordx4 v5, s[36:39], s100 offen offset:2048 lds\n"
"ds_read_b128 v[144:147], v10 offset:0\n"
"ds_read_b128 v[148:151], v10 offset:1024\n"
"ds_read_b128 v[152:155], v10 offset:2048\n"
"buffer_load_dwordx4 v6, s[36:39], s100 offen offset:3072 lds\n"
"ds_read_b128 v[156:159], v10 offset:3072\n"
"ds_read_b64_tr_b16 a[0:1], v7 offset:0\n"
"ds_read_b64_tr_b16 a[2:3], v7 offset:2048\n"
"s_add_u32 m0, s13, 98304\n"
"s_add_u32 s100, s78, 1048576\n"
"buffer_load_dwordx4 v3, s[36:39], s100 offen offset:0 lds\n"
"ds_read_b64_tr_b16 a[4:5], v7 offset:16384\n"
"ds_read_b64_tr_b16 a[6:7], v7 offset:18432\n"
"ds_read_b64_tr_b16 a[8:9], v7 offset:32768\n"
"buffer_load_dwordx4 v4, s[36:39], s100 offen offset:1024 lds\n"
"ds_read_b64_tr_b16 a[10:11], v7 offset:34816\n"
"ds_read_b64_tr_b16 a[36:37], v7 offset:4096\n"
"ds_read_b64_tr_b16 a[38:39], v7 offset:6144\n"
"buffer_load_dwordx4 v5, s[36:39], s100 offen offset:2048 lds\n"
"ds_read_b64_tr_b16 a[40:41], v7 offset:20480\n"
"ds_read_b64_tr_b16 a[42:43], v7 offset:22528\n"
"ds_read_b64_tr_b16 a[44:45], v7 offset:36864\n"
"buffer_load_dwordx4 v6, s[36:39], s100 offen offset:3072 lds\n"
"ds_read_b64_tr_b16 a[46:47], v7 offset:38912\n"
"ds_read_b64_tr_b16 a[72:73], v7 offset:8192\n"
"ds_read_b64_tr_b16 a[74:75], v7 offset:10240\n"
"s_add_u32 m0, s13, 114688\n"
"s_add_u32 s100, s78, 2621440\n"
"buffer_load_dwordx4 v3, s[36:39], s100 offen offset:0 lds\n"
"ds_read_b64_tr_b16 a[76:77], v7 offset:24576\n"
"ds_read_b64_tr_b16 a[78:79], v7 offset:26624\n"
"ds_read_b64_tr_b16 a[80:81], v7 offset:40960\n"
"buffer_load_dwordx4 v4, s[36:39], s100 offen offset:1024 lds\n"
"ds_read_b64_tr_b16 a[82:83], v7 offset:43008\n"
"ds_read_b64_tr_b16 a[108:109], v7 offset:12288\n"
"ds_read_b64_tr_b16 a[110:111], v7 offset:14336\n"
"buffer_load_dwordx4 v5, s[36:39], s100 offen offset:2048 lds\n"
"ds_read_b64_tr_b16 a[112:113], v7 offset:28672\n"
"ds_read_b64_tr_b16 a[114:115], v7 offset:30720\n"
"ds_read_b64_tr_b16 a[116:117], v7 offset:45056\n"
"buffer_load_dwordx4 v6, s[36:39], s100 offen offset:3072 lds\n"
"ds_read_b64_tr_b16 a[118:119], v7 offset:47104\n"
".Lcn_L_tkchk:\n"
"s_waitcnt vmcnt(20)\n"
"v_cmp_ne_u32_e32 vcc, s66, v72\n"
"s_and_b64 vcc, vcc, s[96:97]\n"
"s_cbranch_vccnz .Lcn_R_tk\n"
".Lcn_L_tkok:\n"
"s_waitcnt lgkmcnt(0)\n"
"s_barrier\n"
"s_add_u32 m0, s13, 65536\n"
"s_add_u32 s100, s78, 4194304\n"
"buffer_load_dwordx4 v3, s[36:39], s100 offen offset:0 lds\n"
"buffer_load_dwordx4 v4, s[36:39], s100 offen offset:1024 lds\n"
"buffer_load_dwordx4 v5, s[36:39], s100 offen offset:2048 lds\n"
"buffer_load_dwordx4 v6, s[36:39], s100 offen offset:3072 lds\n"
"s_add_u32 m0, s13, 0\n"
"s_add_u32 s76, s24, 32768\n"
"buffer_load_dwordx4 v1, s[28:31], s24 offen sc1 lds\n"
"buffer_load_dwordx4 v2, s[28:31], s24 offen offset:1024 sc1 lds\n"
"s_add_u32 m0, s13, 2048\n"
"s_nop 0\n"
"buffer_load_dwordx4 v1, s[28:31], s76 offen sc1 lds\n"
"buffer_load_dwordx4 v2, s[28:31], s76 offen offset:1024 sc1 lds\n"
"s_add_u32 m0, s13, 16384\n"
"s_add_u32 s76, s25, 32768\n"
"buffer_load_dwordx4 v1, s[28:31], s25 offen sc1 lds\n"
"buffer_load_dwordx4 v2, s[28:31], s25 offen offset:1024 sc1 lds\n"
"s_add_u32 m0, s13, 18432\n"
"s_nop 0\n"
"buffer_load_dwordx4 v1, s[28:31], s76 offen sc1 lds\n"
"buffer_load_dwordx4 v2, s[28:31], s76 offen offset:1024 sc1 lds\n"
"s_waitcnt vmcnt(20)\n"
"s_barrier\n"
"ds_read_b64_tr_b16 a[12:13], v8 offset:32768\n"
"ds_read_b64_tr_b16 a[14:15], v8 offset:34816\n"
"ds_read_b64_tr_b16 a[16:17], v8 offset:49152\n"
"ds_read_b64_tr_b16 a[18:19], v8 offset:51200\n"
"ds_read_b64_tr_b16 a[20:21], v7 offset:49152\n"
"ds_read_b64_tr_b16 a[22:23], v7 offset:51200\n"
"ds_read_b64_tr_b16 a[48:49], v8 offset:36864\n"
"ds_read_b64_tr_b16 a[50:51], v8 offset:38912\n"
"ds_read_b64_tr_b16 a[52:53], v8 offset:53248\n"
"ds_read_b64_tr_b16 a[54:55], v8 offset:55296\n"
"ds_read_b64_tr_b16 a[56:57], v7 offset:53248\n"
"ds_read_b64_tr_b16 a[58:59], v7 offset:55296\n"
"ds_read_b64_tr_b16 a[84:85], v8 offset:40960\n"
"ds_read_b64_tr_b16 a[86:87], v8 offset:43008\n"
"ds_read_b64_tr_b16 a[88:89], v8 offset:57344\n"
"ds_read_b64_tr_b16 a[90:91], v8 offset:59392\n"
"ds_read_b64_tr_b16 a[92:93], v7 offset:57344\n"
"ds_read_b64_tr_b16 a[94:95], v7 offset:59392\n"
"ds_read_b64_tr_b16 a[120:121], v8 offset:45056\n"
"ds_read_b64_tr_b16 a[122:123], v8 offset:47104\n"
"ds_read_b64_tr_b16 a[124:125], v8 offset:61440\n"
"ds_read_b64_tr_b16 a[126:127], v8 offset:63488\n"
"ds_read_b64_tr_b16 a[128:129], v7 offset:61440\n"
"ds_read_b64_tr_b16 a[130:131], v7 offset:63488\n"
"s_waitcnt vmcnt(0)\n"
".Lcn_L_a0chk:\n"
"ds_read_b128 v[160:163], v10 offset:0\n"
"ds_read_b128 v[164:167], v10 offset:1024\n"
"ds_read_b128 v[168:171], v10 offset:2048\n"
"ds_read_b128 v[172:175], v10 offset:3072\n"
"ds_read_b128 v[176:179], v10 offset:16384\n"
"ds_read_b128 v[180:183], v10 offset:17408\n"
"ds_read_b128 v[184:187], v10 offset:18432\n"
"ds_read_b128 v[188:191], v10 offset:19456\n"
"s_waitcnt lgkmcnt(0)\n"
"v_max3_u32 v248, v160, v161, v162\n"
"v_max3_u32 v249, v163, v164, v165\n"
"v_max3_u32 v248, v248, v166, v167\n"
"v_max3_u32 v249, v249, v168, v169\n"
"v_max3_u32 v248, v248, v170, v171\n"
"v_max3_u32 v249, v249, v172, v173\n"
"v_max3_u32 v248, v248, v174, v175\n"
"v_max3_u32 v249, v249, v176, v177\n"
"v_max3_u32 v248, v248, v178, v179\n"
"v_max3_u32 v249, v249, v180, v181\n"
"v_max3_u32 v248, v248, v182, v183\n"
"v_max3_u32 v249, v249, v184, v185\n"
"v_max3_u32 v248, v248, v186, v187\n"
"v_max3_u32 v249, v249, v188, v189\n"
"v_max3_u32 v248, v248, v190, v191\n"
"v_max_u32_e64 v248, v248, v249\n"
"v_cmp_eq_u32_e64 vcc, -1, v248\n"
"s_cbranch_vccnz .Lcn_R_a0\n"
".Lcn_L_a0ok:\n"
"s_barrier\n"
"ds_read_b64_tr_b16 a[24:25], v8 offset:0\n"
"ds_read_b64_tr_b16 a[26:27], v8 offset:2048\n"
"ds_read_b64_tr_b16 a[28:29], v8 offset:16384\n"
"ds_read_b64_tr_b16 a[30:31], v8 offset:18432\n"
"ds_read_b64_tr_b16 a[32:33], v7 offset:32768\n"
"ds_read_b64_tr_b16 a[34:35], v7 offset:34816\n"
"ds_read_b128 v[64:67], v224 offset:0\n"
"ds_read_b128 v[68:71], v224 offset:2048\n"
"ds_read_b128 v[72:75], v227 offset:0\n"
"ds_read_b128 v[76:79], v225 offset:1024\n"
"ds_read_b128 v[80:83], v225 offset:3072\n"
"ds_read_b128 v[84:87], v228 offset:1024\n"
"ds_read_b128 v[88:91], v224 offset:1024\n"
"ds_read_b128 v[92:95], v224 offset:3072\n"
".p2align 3\n"
"s_waitcnt lgkmcnt(7)\n"
"v_mfma_f32_16x16x32_bf16 v[32:35], a[0:3], v[64:67], 0\n"
"ds_read_b128 v[96:99], v227 offset:1024\n"
"s_waitcnt lgkmcnt(7)\n"
"v_mfma_f32_16x16x32_bf16 v[32:35], a[4:7], v[68:71], v[32:35]\n"
"ds_read_b128 v[100:103], v226 offset:0\n"
"s_waitcnt lgkmcnt(7)\n"
"v_mfma_f32_16x16x32_bf16 v[32:35], a[8:11], v[72:75], v[32:35]\n"
"ds_read_b128 v[104:107], v226 offset:2048\n"
"s_waitcnt lgkmcnt(7)\n"
"v_mfma_f32_16x16x32_bf16 v[32:35], a[12:15], v[76:79], v[32:35]\n"
"ds_read_b128 v[108:111], v229 offset:0\n"
"s_waitcnt lgkmcnt(4)\n"
"v_mfma_f32_16x16x32_bf16 v[32:35], a[16:19], v[80:83], v[32:35]\n"
"ds_read_b128 v[112:115], v227 offset:2048\n"
"v_mfma_f32_16x16x32_bf16 v[32:35], a[20:23], v[84:87], v[32:35]\n"
"ds_read_b128 v[116:119], v228 offset:3072\n"
"v_mfma_f32_16x16x32_bf16 v[32:35], a[24:27], v[88:91], v[32:35]\n"
"s_add_u32 m0, s13, 32768\n"
"s_add_u32 s76, s26, 32768\n"
"v_mfma_f32_16x16x32_bf16 v[32:35], a[28:31], v[92:95], v[32:35]\n"
"buffer_load_dwordx4 v1, s[28:31], s26 offen sc1 lds\n"
"s_waitcnt lgkmcnt(2)\n"
"v_mfma_f32_16x16x32_bf16 v[32:35], a[32:35], v[96:99], v[32:35]\n"
"ds_read_b128 v[120:123], v227 offset:3072\n"
"v_mfma_f32_16x16x32_bf16 v[36:39], a[0:3], v[88:91], 0\n"
"ds_read_b64_tr_b16 a[60:61], v8 offset:4096\n"
"v_mfma_f32_16x16x32_bf16 v[36:39], a[4:7], v[92:95], v[36:39]\n"
"ds_read_b64_tr_b16 a[62:63], v8 offset:6144\n"
"v_mfma_f32_16x16x32_bf16 v[36:39], a[8:11], v[96:99], v[36:39]\n"
"buffer_load_dwordx4 v2, s[28:31], s26 offen offset:1024 sc1 lds\n"
"v_mfma_f32_16x16x32_bf16 v[36:39], a[12:15], v[64:67], v[36:39]\n"
"s_add_u32 m0, s13, 34816\n"
"v_mfma_f32_16x16x32_bf16 v[36:39], a[16:19], v[68:71], v[36:39]\n"
"ds_read_b64_tr_b16 a[64:65], v8 offset:20480\n"
"v_mfma_f32_16x16x32_bf16 v[36:39], a[20:23], v[72:75], v[36:39]\n"
"ds_read_b64_tr_b16 a[66:67], v8 offset:22528\n"
"v_mfma_f32_16x16x32_bf16 v[36:39], a[24:27], v[100:103], v[36:39]\n"
"buffer_load_dwordx4 v1, s[28:31], s76 offen sc1 lds\n"
"v_mfma_f32_16x16x32_bf16 v[36:39], a[28:31], v[104:107], v[36:39]\n"
"ds_read_b64_tr_b16 a[68:69], v7 offset:36864\n"
"v_mfma_f32_16x16x32_bf16 v[36:39], a[32:35], v[108:111], v[36:39]\n"
"ds_read_b128 v[124:127], v229 offset:2048\n"
"v_mfma_f32_16x16x32_bf16 v[40:43], a[0:3], v[68:71], 0\n"
"ds_read_b128 v[128:131], v224 offset:7168\n"
"v_mfma_f32_16x16x32_bf16 v[40:43], a[4:7], v[72:75], v[40:43]\n"
"ds_read_b128 v[132:135], v227 offset:5120\n"
"s_waitcnt lgkmcnt(4)\n"
"v_mfma_f32_16x16x32_bf16 v[40:43], a[8:11], v[112:115], v[40:43]\n"
"ds_read_b128 v[76:79], v227 offset:7168\n"
"v_mfma_f32_16x16x32_bf16 v[40:43], a[12:15], v[80:83], v[40:43]\n"
"ds_read_b128 v[88:91], v224 offset:6144\n"
"v_mfma_f32_16x16x32_bf16 v[40:43], a[16:19], v[84:87], v[40:43]\n"
"ds_read_b128 v[64:67], v227 offset:4096\n"
"v_mfma_f32_16x16x32_bf16 v[40:43], a[20:23], v[116:119], v[40:43]\n"
"ds_read_b128 v[100:103], v227 offset:6144\n"
"v_mfma_f32_16x16x32_bf16 v[40:43], a[24:27], v[92:95], v[40:43]\n"
"ds_read_b64_tr_b16 a[70:71], v7 offset:38912\n"
"v_mfma_f32_16x16x32_bf16 v[40:43], a[28:31], v[96:99], v[40:43]\n"
"ds_read_b128 v[80:83], v226 offset:6144\n"
"v_mfma_f32_16x16x32_bf16 v[40:43], a[32:35], v[120:123], v[40:43]\n"
"ds_read_b128 v[84:87], v229 offset:4096\n"
"v_mfma_f32_16x16x32_bf16 v[44:47], a[0:3], v[92:95], 0\n"
"buffer_load_dwordx4 v2, s[28:31], s76 offen offset:1024 sc1 lds\n"
"v_mfma_f32_16x16x32_bf16 v[44:47], a[4:7], v[96:99], v[44:47]\n"
"ds_read_b128 v[116:119], v229 offset:6144\n"
"v_mfma_f32_16x16x32_bf16 v[44:47], a[8:11], v[120:123], v[44:47]\n"
"s_add_u32 m0, s13, 49152\n"
"s_add_u32 s76, s27, 32768\n"
"v_mfma_f32_16x16x32_bf16 v[44:47], a[12:15], v[68:71], v[44:47]\n"
"ds_read_b128 v[92:95], v225 offset:7168\n"
"v_mfma_f32_16x16x32_bf16 v[44:47], a[16:19], v[72:75], v[44:47]\n"
"buffer_load_dwordx4 v1, s[28:31], s27 offen sc1 lds\n"
"v_mfma_f32_16x16x32_bf16 v[44:47], a[20:23], v[112:115], v[44:47]\n"
"ds_read_b128 v[96:99], v228 offset:5120\n"
"v_mfma_f32_16x16x32_bf16 v[44:47], a[24:27], v[104:107], v[44:47]\n"
"ds_read_b128 v[120:123], v228 offset:7168\n"
"v_mfma_f32_16x16x32_bf16 v[44:47], a[28:31], v[108:111], v[44:47]\n"
"ds_read_b64_tr_b16 a[96:97], v8 offset:8192\n"
"s_waitcnt lgkmcnt(3)\n"
"v_mfma_f32_16x16x32_bf16 v[44:47], a[32:35], v[124:127], v[44:47]\n"
"buffer_load_dwordx4 v2, s[28:31], s27 offen offset:1024 sc1 lds\n"
"v_mfma_f32_16x16x32_bf16 v[44:47], a[36:39], v[128:131], v[44:47]\n"
"s_add_u32 m0, s13, 51200\n"
"v_mfma_f32_16x16x32_bf16 v[44:47], a[40:43], v[132:135], v[44:47]\n"
"ds_read_b128 v[68:71], v224 offset:5120\n"
"v_mfma_f32_16x16x32_bf16 v[44:47], a[44:47], v[76:79], v[44:47]\n"
"ds_read_b64_tr_b16 a[98:99], v8 offset:10240\n"
"v_mfma_f32_16x16x32_bf16 v[44:47], a[48:51], v[88:91], v[44:47]\n"
"ds_read_b128 v[72:75], v224 offset:4096\n"
"v_mfma_f32_16x16x32_bf16 v[44:47], a[52:55], v[64:67], v[44:47]\n"
"buffer_load_dwordx4 v1, s[28:31], s76 offen sc1 lds\n"
"v_mfma_f32_16x16x32_bf16 v[44:47], a[56:59], v[100:103], v[44:47]\n"
"ds_read_b64_tr_b16 a[100:101], v8 offset:24576\n"
"v_mfma_f32_16x16x32_bf16 v[44:47], a[60:63], v[80:83], v[44:47]\n"
"ds_read_b128 v[112:115], v226 offset:4096\n"
"v_mfma_f32_16x16x32_bf16 v[44:47], a[64:67], v[84:87], v[44:47]\n"
"ds_read_b64_tr_b16 a[102:103], v8 offset:26624\n"
"v_mfma_f32_16x16x32_bf16 v[44:47], a[68:71], v[116:119], v[44:47]\n"
"buffer_load_dwordx4 v2, s[28:31], s76 offen offset:1024 sc1 lds\n"
"v_mfma_f32_16x16x32_bf16 v[40:43], a[36:39], v[88:91], v[40:43]\n"
"s_add_u32 m0, s13, 131072\n"
"s_add_u32 s100, s12, 0\n"
"v_mfma_f32_16x16x32_bf16 v[40:43], a[40:43], v[64:67], v[40:43]\n"
"ds_read_b64_tr_b16 a[104:105], v7 offset:40960\n"
"v_mfma_f32_16x16x32_bf16 v[40:43], a[44:47], v[100:103], v[40:43]\n"
"ds_read_b64_tr_b16 a[106:107], v7 offset:43008\n"
"v_mfma_f32_16x16x32_bf16 v[40:43], a[48:51], v[92:95], v[40:43]\n"
"ds_read_b128 v[104:107], v225 offset:5120\n"
"s_waitcnt lgkmcnt(3)\n"
"v_mfma_f32_16x16x32_bf16 v[40:43], a[52:55], v[96:99], v[40:43]\n"
"buffer_load_dwordx4 v13, s[40:43], s100 offen offset:0 lds\n"
"v_mfma_f32_16x16x32_bf16 v[40:43], a[56:59], v[120:123], v[40:43]\n"
"ds_read_b64_tr_b16 a[132:133], v8 offset:12288\n"
"v_mfma_f32_16x16x32_bf16 v[40:43], a[60:63], v[128:131], v[40:43]\n"
"ds_read_b64_tr_b16 a[134:135], v8 offset:14336\n"
"v_mfma_f32_16x16x32_bf16 v[40:43], a[64:67], v[132:135], v[40:43]\n"
"ds_read_b64_tr_b16 a[136:137], v8 offset:28672\n"
"v_mfma_f32_16x16x32_bf16 v[40:43], a[68:71], v[76:79], v[40:43]\n"
"ds_read_b64_tr_b16 a[138:139], v8 offset:30720\n"
"v_mfma_f32_16x16x32_bf16 v[36:39], a[36:39], v[68:71], v[36:39]\n"
"buffer_load_dwordx4 v14, s[40:43], s100 offen offset:1024 lds\n"
"v_mfma_f32_16x16x32_bf16 v[36:39], a[40:43], v[128:131], v[36:39]\n"
"ds_read_b128 v[108:111], v224 offset:8192\n"
"v_mfma_f32_16x16x32_bf16 v[36:39], a[44:47], v[132:135], v[36:39]\n"
"ds_read_b128 v[124:127], v224 offset:10240\n"
"v_mfma_f32_16x16x32_bf16 v[36:39], a[48:51], v[72:75], v[36:39]\n"
"ds_read_b128 v[116:119], v227 offset:8192\n"
"v_mfma_f32_16x16x32_bf16 v[36:39], a[52:55], v[88:91], v[36:39]\n"
"ds_read_b128 v[100:103], v225 offset:9216\n"
"v_mfma_f32_16x16x32_bf16 v[36:39], a[56:59], v[64:67], v[36:39]\n"
"buffer_load_dwordx4 v15, s[40:43], s100 offen offset:2048 lds\n"
"v_mfma_f32_16x16x32_bf16 v[36:39], a[60:63], v[112:115], v[36:39]\n"
"ds_read_b128 v[120:123], v225 offset:11264\n"
"v_mfma_f32_16x16x32_bf16 v[36:39], a[64:67], v[80:83], v[36:39]\n"
"ds_read_b64_tr_b16 a[140:141], v7 offset:45056\n"
"v_mfma_f32_16x16x32_bf16 v[36:39], a[68:71], v[84:87], v[36:39]\n"
"ds_read_b128 v[76:79], v228 offset:9216\n"
"v_mfma_f32_16x16x32_bf16 v[32:35], a[36:39], v[72:75], v[32:35]\n"
"ds_read_b64_tr_b16 a[142:143], v7 offset:47104\n"
"v_mfma_f32_16x16x32_bf16 v[32:35], a[40:43], v[88:91], v[32:35]\n"
"ds_read_b128 v[112:115], v224 offset:9216\n"
"v_mfma_f32_16x16x32_bf16 v[32:35], a[44:47], v[64:67], v[32:35]\n"
"ds_read_b128 v[80:83], v224 offset:11264\n"
"s_waitcnt lgkmcnt(4)\n"
"v_mfma_f32_16x16x32_bf16 v[32:35], a[48:51], v[104:107], v[32:35]\n"
"buffer_load_dwordx4 v16, s[40:43], s100 offen offset:3072 lds\n"
"v_mfma_f32_16x16x32_bf16 v[32:35], a[52:55], v[92:95], v[32:35]\n"
"s_add_u32 m0, s13, 147456\n"
"s_add_u32 s100, s12, 131072\n"
"v_mfma_f32_16x16x32_bf16 v[32:35], a[56:59], v[96:99], v[32:35]\n"
"ds_read_b128 v[84:87], v227 offset:9216\n"
"v_mfma_f32_16x16x32_bf16 v[32:35], a[60:63], v[68:71], v[32:35]\n"
"ds_read_b128 v[72:75], v226 offset:8192\n"
"v_mfma_f32_16x16x32_bf16 v[32:35], a[64:67], v[128:131], v[32:35]\n"
"ds_read_b128 v[88:91], v226 offset:10240\n"
"v_mfma_f32_16x16x32_bf16 v[32:35], a[68:71], v[132:135], v[32:35]\n"
"buffer_load_dwordx4 v13, s[40:43], s100 offen offset:0 lds\n"
"v_mfma_f32_16x16x32_bf16 v[32:35], a[72:75], v[108:111], v[32:35]\n"
"s_waitcnt vmcnt(9)\n"
"ds_read_b128 v[160:163], v10 offset:32768\n"
"v_mfma_f32_16x16x32_bf16 v[32:35], a[76:79], v[124:127], v[32:35]\n"
"ds_read_b128 v[64:67], v229 offset:8192\n"
"v_mfma_f32_16x16x32_bf16 v[32:35], a[80:83], v[116:119], v[32:35]\n"
"ds_read_b128 v[164:167], v10 offset:33792\n"
"v_mfma_f32_16x16x32_bf16 v[32:35], a[84:87], v[100:103], v[32:35]\n"
"ds_read_b128 v[168:171], v10 offset:34816\n"
"v_mfma_f32_16x16x32_bf16 v[32:35], a[88:91], v[120:123], v[32:35]\n"
"buffer_load_dwordx4 v14, s[40:43], s100 offen offset:1024 lds\n"
"s_waitcnt lgkmcnt(3)\n"
"v_mfma_f32_16x16x32_bf16 v[32:35], a[92:95], v[76:79], v[32:35]\n"
"ds_read_b128 v[172:175], v10 offset:35840\n"
"v_mfma_f32_16x16x32_bf16 v[32:35], a[96:99], v[112:115], v[32:35]\n"
"ds_read_b128 v[104:107], v227 offset:10240\n"
"v_mfma_f32_16x16x32_bf16 v[32:35], a[100:103], v[80:83], v[32:35]\n"
"ds_read_b128 v[92:95], v228 offset:11264\n"
"v_mfma_f32_16x16x32_bf16 v[32:35], a[104:107], v[84:87], v[32:35]\n"
"s_barrier\n"
"v_mfma_f32_16x16x32_bf16 v[36:39], a[72:75], v[112:115], v[36:39]\n"
"buffer_load_dwordx4 v15, s[40:43], s100 offen offset:2048 lds\n"
"v_mfma_f32_16x16x32_bf16 v[36:39], a[76:79], v[80:83], v[36:39]\n"
"ds_read_b128 v[96:99], v227 offset:11264\n"
"v_mfma_f32_16x16x32_bf16 v[36:39], a[80:83], v[84:87], v[36:39]\n"
"v_max3_u32 v248, v160, v161, v162\n"
"s_waitcnt lgkmcnt(1)\n"
"v_max3_u32 v249, v163, v164, v165\n"
"v_mfma_f32_16x16x32_bf16 v[36:39], a[84:87], v[108:111], v[36:39]\n"
"v_max3_u32 v248, v248, v166, v167\n"
"v_max3_u32 v249, v249, v168, v169\n"
"v_mfma_f32_16x16x32_bf16 v[36:39], a[88:91], v[124:127], v[36:39]\n"
"v_max3_u32 v248, v248, v170, v171\n"
"v_max3_u32 v249, v249, v172, v173\n"
"v_mfma_f32_16x16x32_bf16 v[36:39], a[92:95], v[116:119], v[36:39]\n"
"buffer_load_dwordx4 v16, s[40:43], s100 offen offset:3072 lds\n"
"v_mfma_f32_16x16x32_bf16 v[36:39], a[96:99], v[72:75], v[36:39]\n"
"s_add_u32 m0, s13, 65536\n"
"s_add_u32 s76, s32, 32768\n"
"v_mfma_f32_16x16x32_bf16 v[36:39], a[100:103], v[88:91], v[36:39]\n"
"v_max3_u32 v248, v248, v174, v175\n"
"v_max_u32_e64 v248, v248, v249\n"
"v_mfma_f32_16x16x32_bf16 v[36:39], a[104:107], v[64:67], v[36:39]\n"
"v_cmp_eq_u32_e64 vcc, -1, v248\n"
"s_cbranch_vccnz .Lcn_R_a2\n"
".Lcn_L_a2ok:\n"
"v_mfma_f32_16x16x32_bf16 v[40:43], a[72:75], v[124:127], v[40:43]\n"
"buffer_load_dwordx4 v1, s[28:31], s32 offen sc1 lds\n"
"v_mfma_f32_16x16x32_bf16 v[40:43], a[76:79], v[116:119], v[40:43]\n"
"ds_read_b128 v[68:71], v229 offset:10240\n"
"v_mfma_f32_16x16x32_bf16 v[40:43], a[80:83], v[104:107], v[40:43]\n"
"ds_read_b128 v[128:131], v224 offset:15360\n"
"v_mfma_f32_16x16x32_bf16 v[40:43], a[84:87], v[120:123], v[40:43]\n"
"s_waitcnt vmcnt(9)\n"
"ds_read_b128 v[160:163], v10 offset:49152\n"
"v_mfma_f32_16x16x32_bf16 v[40:43], a[88:91], v[76:79], v[40:43]\n"
"buffer_load_dwordx4 v2, s[28:31], s32 offen offset:1024 sc1 lds\n"
"v_mfma_f32_16x16x32_bf16 v[40:43], a[92:95], v[92:95], v[40:43]\n"
"ds_read_b128 v[132:135], v227 offset:13312\n"
"v_mfma_f32_16x16x32_bf16 v[40:43], a[96:99], v[80:83], v[40:43]\n"
"ds_read_b128 v[164:167], v10 offset:50176\n"
"v_mfma_f32_16x16x32_bf16 v[40:43], a[100:103], v[84:87], v[40:43]\n"
"s_add_u32 m0, s13, 67584\n"
"s_waitcnt lgkmcnt(2)\n"
"v_mfma_f32_16x16x32_bf16 v[40:43], a[104:107], v[96:99], v[40:43]\n"
"ds_read_b128 v[100:103], v227 offset:15360\n"
"v_mfma_f32_16x16x32_bf16 v[44:47], a[72:75], v[80:83], v[44:47]\n"
"ds_read_b128 v[168:171], v10 offset:51200\n"
"v_mfma_f32_16x16x32_bf16 v[44:47], a[76:79], v[84:87], v[44:47]\n"
"ds_read_b128 v[112:115], v224 offset:14336\n"
"v_mfma_f32_16x16x32_bf16 v[44:47], a[80:83], v[96:99], v[44:47]\n"
"ds_read_b128 v[172:175], v10 offset:52224\n"
"v_mfma_f32_16x16x32_bf16 v[44:47], a[84:87], v[124:127], v[44:47]\n"
"ds_read_b128 v[108:111], v227 offset:12288\n"
"v_mfma_f32_16x16x32_bf16 v[44:47], a[88:91], v[116:119], v[44:47]\n"
"ds_read_b128 v[72:75], v227 offset:14336\n"
"v_mfma_f32_16x16x32_bf16 v[44:47], a[92:95], v[104:107], v[44:47]\n"
"buffer_load_dwordx4 v1, s[28:31], s76 offen sc1 lds\n"
"v_mfma_f32_16x16x32_bf16 v[44:47], a[96:99], v[88:91], v[44:47]\n"
"ds_read_b128 v[120:123], v226 offset:14336\n"
"v_mfma_f32_16x16x32_bf16 v[44:47], a[100:103], v[64:67], v[44:47]\n"
"ds_read_b128 v[76:79], v229 offset:12288\n"
"v_mfma_f32_16x16x32_bf16 v[44:47], a[104:107], v[68:71], v[44:47]\n"
"ds_read_b128 v[92:95], v229 offset:14336\n"
"v_mfma_f32_16x16x32_bf16 v[44:47], a[108:111], v[128:131], v[44:47]\n"
"buffer_load_dwordx4 v2, s[28:31], s76 offen offset:1024 sc1 lds\n"
"s_waitcnt lgkmcnt(3)\n"
"v_mfma_f32_16x16x32_bf16 v[44:47], a[112:115], v[132:135], v[44:47]\n"
"s_add_u32 m0, s13, 81920\n"
"s_add_u32 s100, s12, 262144\n"
"v_mfma_f32_16x16x32_bf16 v[44:47], a[116:119], v[100:103], v[44:47]\n"
"v_max3_u32 v248, v160, v161, v162\n"
"v_max3_u32 v249, v163, v164, v165\n"
"v_mfma_f32_16x16x32_bf16 v[44:47], a[120:123], v[112:115], v[44:47]\n"
"v_add_u32_e32 v230, s33, v1\n"
"v_add_u32_e32 v231, s33, v19\n"
"v_mfma_f32_16x16x32_bf16 v[44:47], a[124:127], v[108:111], v[44:47]\n"
"ds_read_b128 v[80:83], v225 offset:15360\n"
"v_mfma_f32_16x16x32_bf16 v[44:47], a[128:131], v[72:75], v[44:47]\n"
"v_max3_u32 v248, v248, v166, v167\n"
"v_max3_u32 v249, v249, v168, v169\n"
"buffer_load_dwordx4 v13, s[40:43], s100 offen offset:0 lds\n"
"s_waitcnt lgkmcnt(1)\n"
"v_mfma_f32_16x16x32_bf16 v[44:47], a[132:135], v[120:123], v[44:47]\n"
"v_add_u32_e32 v232, s33, v20\n"
"v_add_u32_e32 v233, s72, v1\n"
"ds_read_b128 v[84:87], v228 offset:13312\n"
"v_mfma_f32_16x16x32_bf16 v[44:47], a[136:139], v[76:79], v[44:47]\n"
"v_max3_u32 v248, v248, v170, v171\n"
"v_max3_u32 v249, v249, v172, v173\n"
"v_add_u32_e32 v234, s72, v19\n"
"v_add_u32_e32 v235, s72, v20\n"
"v_mfma_f32_16x16x32_bf16 v[44:47], a[140:143], v[92:95], v[44:47]\n"
"ds_read_b128 v[96:99], v228 offset:15360\n"
"v_max3_u32 v248, v248, v174, v175\n"
"v_max_u32_e64 v248, v248, v249\n"
"v_mfma_f32_16x16x32_bf16 v[40:43], a[108:111], v[112:115], v[40:43]\n"
"v_cmp_eq_u32_e64 vcc, -1, v248\n"
"s_cbranch_vccnz .Lcn_R_a3\n"
".Lcn_L_a3ok:\n"
"v_mfma_f32_16x16x32_bf16 v[40:43], a[112:115], v[108:111], v[40:43]\n"
"buffer_load_dwordx4 v14, s[40:43], s100 offen offset:1024 lds\n"
"ds_read_b128 v[124:127], v224 offset:13312\n"
"v_mfma_f32_16x16x32_bf16 v[40:43], a[116:119], v[72:75], v[40:43]\n"
"ds_read_b128 v[116:119], v224 offset:12288\n"
"s_waitcnt lgkmcnt(3)\n"
"v_mfma_f32_16x16x32_bf16 v[40:43], a[120:123], v[80:83], v[40:43]\n"
"ds_read_b128 v[104:107], v226 offset:12288\n"
"v_mfma_f32_16x16x32_bf16 v[40:43], a[124:127], v[84:87], v[40:43]\n"
"s_barrier\n"
"s_waitcnt lgkmcnt(3)\n"
"v_mfma_f32_16x16x32_bf16 v[40:43], a[128:131], v[96:99], v[40:43]\n"
"ds_read_b128 v[88:91], v225 offset:13312\n"
"v_mfma_f32_16x16x32_bf16 v[40:43], a[132:135], v[128:131], v[40:43]\n"
"buffer_load_dwordx4 v15, s[40:43], s100 offen offset:2048 lds\n"
"v_mfma_f32_16x16x32_bf16 v[40:43], a[136:139], v[132:135], v[40:43]\n"
"v_mfma_f32_16x16x32_bf16 v[40:43], a[140:143], v[100:103], v[40:43]\n"
"s_waitcnt lgkmcnt(1)\n"
"v_mfma_f32_16x16x32_bf16 v[36:39], a[108:111], v[124:127], v[36:39]\n"
"ds_read_b128 v[64:67], v230 offset:0\n"
"v_mfma_f32_16x16x32_bf16 v[36:39], a[112:115], v[128:131], v[36:39]\n"
"ds_read_b128 v[68:71], v230 offset:2048\n"
"v_mfma_f32_16x16x32_bf16 v[36:39], a[116:119], v[132:135], v[36:39]\n"
"buffer_load_dwordx4 v16, s[40:43], s100 offen offset:3072 lds\n"
"v_mfma_f32_16x16x32_bf16 v[36:39], a[120:123], v[116:119], v[36:39]\n"
"s_add_u32 m0, s13, 98304\n"
"s_add_u32 s100, s12, 393216\n"
"v_mfma_f32_16x16x32_bf16 v[36:39], a[124:127], v[112:115], v[36:39]\n"
"ds_read_b128 v[92:95], v233 offset:0\n"
"v_mfma_f32_16x16x32_bf16 v[36:39], a[128:131], v[108:111], v[36:39]\n"
"buffer_load_dwordx4 v13, s[40:43], s100 offen offset:0 lds\n"
"v_mfma_f32_16x16x32_bf16 v[36:39], a[132:135], v[104:107], v[36:39]\n"
"ds_read_b128 v[72:75], v231 offset:1024\n"
"v_mfma_f32_16x16x32_bf16 v[36:39], a[136:139], v[120:123], v[36:39]\n"
"ds_read_b128 v[96:99], v231 offset:3072\n"
"v_mfma_f32_16x16x32_bf16 v[36:39], a[140:143], v[76:79], v[36:39]\n"
"ds_read_b128 v[100:103], v234 offset:1024\n"
"v_mfma_f32_16x16x32_bf16 v[32:35], a[108:111], v[116:119], v[32:35]\n"
"ds_read_b128 v[104:107], v230 offset:1024\n"
"v_mfma_f32_16x16x32_bf16 v[32:35], a[112:115], v[112:115], v[32:35]\n"
"buffer_load_dwordx4 v14, s[40:43], s100 offen offset:1024 lds\n"
"v_mfma_f32_16x16x32_bf16 v[32:35], a[116:119], v[108:111], v[32:35]\n"
"ds_read_b128 v[120:123], v230 offset:3072\n"
"s_waitcnt lgkmcnt(3)\n"
"v_mfma_f32_16x16x32_bf16 v[32:35], a[120:123], v[88:91], v[32:35]\n"
"ds_read_b128 v[76:79], v233 offset:1024\n"
"v_mfma_f32_16x16x32_bf16 v[32:35], a[124:127], v[80:83], v[32:35]\n"
"v_mfma_f32_16x16x32_bf16 v[32:35], a[128:131], v[84:87], v[32:35]\n"
"v_mfma_f32_16x16x32_bf16 v[32:35], a[132:135], v[124:127], v[32:35]\n"
"buffer_load_dwordx4 v15, s[40:43], s100 offen offset:2048 lds\n"
"v_mfma_f32_16x16x32_bf16 v[32:35], a[136:139], v[128:131], v[32:35]\n"
"ds_read_b128 v[116:119], v232 offset:0\n"
"v_mfma_f32_16x16x32_bf16 v[32:35], a[140:143], v[132:135], v[32:35]\n"
"ds_read_b128 v[112:115], v232 offset:2048\n"
"v_mfma_f32_16x16x32_bf16 v[48:51], a[0:3], v[64:67], 0\n"
"ds_read_b128 v[108:111], v235 offset:0\n"
"v_mfma_f32_16x16x32_bf16 v[48:51], a[4:7], v[68:71], v[48:51]\n"
"v_mfma_f32_16x16x32_bf16 v[48:51], a[8:11], v[92:95], v[48:51]\n"
"buffer_load_dwordx4 v16, s[40:43], s100 offen offset:3072 lds\n"
"v_mfma_f32_16x16x32_bf16 v[48:51], a[12:15], v[72:75], v[48:51]\n"
"ds_read_b128 v[88:91], v233 offset:2048\n"
"v_mfma_f32_16x16x32_bf16 v[48:51], a[16:19], v[96:99], v[48:51]\n"
"s_waitcnt vmcnt(8)\n"
"ds_read_b128 v[160:163], v11 offset:0\n"
"s_waitcnt lgkmcnt(2)\n"
"v_mfma_f32_16x16x32_bf16 v[48:51], a[20:23], v[100:103], v[48:51]\n"
"ds_read_b128 v[80:83], v234 offset:3072\n"
"v_mfma_f32_16x16x32_bf16 v[48:51], a[24:27], v[104:107], v[48:51]\n"
"v_mov_b32_e32 v188, -1\n"
"v_mov_b32_e32 v189, -1\n"
"v_mfma_f32_16x16x32_bf16 v[48:51], a[28:31], v[120:123], v[48:51]\n"
"v_mov_b32_e32 v190, -1\n"
"v_mov_b32_e32 v191, -1\n"
"v_mfma_f32_16x16x32_bf16 v[48:51], a[32:35], v[76:79], v[48:51]\n"
"ds_read_b128 v[84:87], v233 offset:3072\n"
"v_mfma_f32_16x16x32_bf16 v[52:55], a[0:3], v[104:107], 0\n"
"ds_read_b128 v[164:167], v11 offset:1024\n"
"v_mfma_f32_16x16x32_bf16 v[52:55], a[4:7], v[120:123], v[52:55]\n"
"buffer_store_dwordx4 v[188:191], v1, s[52:55], s9 offen offset:0 sc1\n"
"v_mfma_f32_16x16x32_bf16 v[52:55], a[8:11], v[76:79], v[52:55]\n"
"ds_read_b128 v[168:171], v11 offset:2048\n"
"v_mfma_f32_16x16x32_bf16 v[52:55], a[12:15], v[64:67], v[52:55]\n"
"ds_read_b128 v[172:175], v11 offset:3072\n"
"v_mfma_f32_16x16x32_bf16 v[52:55], a[16:19], v[68:71], v[52:55]\n"
"v_mfma_f32_16x16x32_bf16 v[52:55], a[20:23], v[92:95], v[52:55]\n"
"v_mfma_f32_16x16x32_bf16 v[52:55], a[24:27], v[116:119], v[52:55]\n"
"v_mfma_f32_16x16x32_bf16 v[52:55], a[28:31], v[112:115], v[52:55]\n"
"v_mfma_f32_16x16x32_bf16 v[52:55], a[32:35], v[108:111], v[52:55]\n"
"ds_read_b128 v[124:127], v235 offset:2048\n"
"v_mfma_f32_16x16x32_bf16 v[56:59], a[0:3], v[68:71], 0\n"
"ds_read_b128 v[128:131], v230 offset:7168\n"
"v_mfma_f32_16x16x32_bf16 v[56:59], a[4:7], v[92:95], v[56:59]\n"
"ds_read_b128 v[132:135], v233 offset:5120\n"
"s_waitcnt lgkmcnt(3)\n"
"v_mfma_f32_16x16x32_bf16 v[56:59], a[8:11], v[88:91], v[56:59]\n"
"buffer_store_dwordx4 v[188:191], v1, s[52:55], s9 offen offset:1024 sc1\n"
"v_mfma_f32_16x16x32_bf16 v[56:59], a[12:15], v[96:99], v[56:59]\n"
"ds_read_b128 v[72:75], v233 offset:7168\n"
"v_mfma_f32_16x16x32_bf16 v[56:59], a[16:19], v[100:103], v[56:59]\n"
"v_cvt_pk_bf16_f32 v192, v32, v33\n"
"v_cvt_pk_bf16_f32 v193, v34, v35\n"
"v_mfma_f32_16x16x32_bf16 v[56:59], a[20:23], v[80:83], v[56:59]\n"
"ds_read_b128 v[104:107], v230 offset:6144\n"
"v_mfma_f32_16x16x32_bf16 v[56:59], a[24:27], v[120:123], v[56:59]\n"
"v_cvt_pk_bf16_f32 v194, v36, v37\n"
"v_cvt_pk_bf16_f32 v195, v38, v39\n"
"v_mfma_f32_16x16x32_bf16 v[56:59], a[28:31], v[76:79], v[56:59]\n"
"ds_read_b128 v[64:67], v233 offset:4096\n"
"v_mfma_f32_16x16x32_bf16 v[56:59], a[32:35], v[84:87], v[56:59]\n"
"ds_read_b128 v[116:119], v233 offset:6144\n"
"v_mfma_f32_16x16x32_bf16 v[60:63], a[0:3], v[120:123], 0\n"
"v_max3_u32 v248, v160, v161, v162\n"
"v_max3_u32 v249, v163, v164, v165\n"
"v_mfma_f32_16x16x32_bf16 v[60:63], a[4:7], v[76:79], v[60:63]\n"
"ds_read_b128 v[96:99], v232 offset:6144\n"
"v_mfma_f32_16x16x32_bf16 v[60:63], a[8:11], v[84:87], v[60:63]\n"
"v_max3_u32 v248, v248, v166, v167\n"
"v_max3_u32 v249, v249, v168, v169\n"
"v_mfma_f32_16x16x32_bf16 v[60:63], a[12:15], v[68:71], v[60:63]\n"
"ds_read_b128 v[100:103], v235 offset:4096\n"
"v_mfma_f32_16x16x32_bf16 v[60:63], a[16:19], v[92:95], v[60:63]\n"
"v_max3_u32 v248, v248, v170, v171\n"
"v_max3_u32 v249, v249, v172, v173\n"
"v_mfma_f32_16x16x32_bf16 v[60:63], a[20:23], v[88:91], v[60:63]\n"
"ds_read_b128 v[80:83], v235 offset:6144\n"
"v_mfma_f32_16x16x32_bf16 v[60:63], a[24:27], v[112:115], v[60:63]\n"
"v_max3_u32 v248, v248, v174, v175\n"
"v_max_u32_e64 v248, v248, v249\n"
"v_mfma_f32_16x16x32_bf16 v[60:63], a[28:31], v[108:111], v[60:63]\n"
"v_cmp_eq_u32_e64 vcc, -1, v248\n"
"s_cbranch_vccnz .Lcn_R_a4\n"
".Lcn_L_a4ok:\n"
"s_waitcnt lgkmcnt(1)\n"
"v_mfma_f32_16x16x32_bf16 v[60:63], a[32:35], v[124:127], v[60:63]\n"
"buffer_store_dwordx4 v[192:195], v1, s[52:55], s8 offen offset:0 sc1\n"
"v_mfma_f32_16x16x32_bf16 v[60:63], a[36:39], v[128:131], v[60:63]\n"
"ds_read_b128 v[120:123], v231 offset:7168\n"
"v_mfma_f32_16x16x32_bf16 v[60:63], a[40:43], v[132:135], v[60:63]\n"
"v_cvt_pk_bf16_f32 v196, v40, v41\n"
"v_cvt_pk_bf16_f32 v197, v42, v43\n"
"v_mfma_f32_16x16x32_bf16 v[60:63], a[44:47], v[72:75], v[60:63]\n"
"ds_read_b128 v[76:79], v234 offset:5120\n"
"v_mfma_f32_16x16x32_bf16 v[60:63], a[48:51], v[104:107], v[60:63]\n"
"v_cvt_pk_bf16_f32 v198, v44, v45\n"
"v_cvt_pk_bf16_f32 v199, v46, v47\n"
"v_mfma_f32_16x16x32_bf16 v[60:63], a[52:55], v[64:67], v[60:63]\n"
"ds_read_b128 v[84:87], v234 offset:7168\n"
"v_mfma_f32_16x16x32_bf16 v[60:63], a[56:59], v[116:119], v[60:63]\n"
"ds_read_b128 v[68:71], v230 offset:5120\n"
"v_mfma_f32_16x16x32_bf16 v[60:63], a[60:63], v[96:99], v[60:63]\n"
"ds_read_b128 v[92:95], v230 offset:4096\n"
"v_mfma_f32_16x16x32_bf16 v[60:63], a[64:67], v[100:103], v[60:63]\n"
"buffer_store_dwordx4 v[196:199], v1, s[52:55], s8 offen offset:1024 sc1\n"
"s_waitcnt lgkmcnt(3)\n"
"v_mfma_f32_16x16x32_bf16 v[60:63], a[68:71], v[80:83], v[60:63]\n"
"s_waitcnt vmcnt(4)\n"
"s_barrier\n"
"v_mfma_f32_16x16x32_bf16 v[56:59], a[36:39], v[104:107], v[56:59]\n"
"ds_read_b128 v[88:91], v232 offset:4096\n"
"v_mfma_f32_16x16x32_bf16 v[56:59], a[40:43], v[64:67], v[56:59]\n"
"ds_read_b64_tr_b16 a[144:145], v12 offset:0\n"
"v_mfma_f32_16x16x32_bf16 v[56:59], a[44:47], v[116:119], v[56:59]\n"
"ds_read_b64_tr_b16 a[146:147], v12 offset:2048\n"
"v_mfma_f32_16x16x32_bf16 v[56:59], a[48:51], v[120:123], v[56:59]\n"
"ds_read_b128 v[112:115], v231 offset:5120\n"
"v_mfma_f32_16x16x32_bf16 v[56:59], a[52:55], v[76:79], v[56:59]\n"
"ds_read_b64_tr_b16 a[148:149], v12 offset:8\n"
"s_waitcnt lgkmcnt(4)\n"
"v_mfma_f32_16x16x32_bf16 v[56:59], a[56:59], v[84:87], v[56:59]\n"
"ds_read_b64_tr_b16 a[150:151], v12 offset:2056\n"
"v_mfma_f32_16x16x32_bf16 v[56:59], a[60:63], v[128:131], v[56:59]\n"
"buffer_store_dwordx4 v[188:191], v1, s[52:55], s10 offen offset:0 sc1\n"
"v_mfma_f32_16x16x32_bf16 v[56:59], a[64:67], v[132:135], v[56:59]\n"
"ds_read_b64_tr_b16 a[152:153], v12 offset:4096\n"
"v_mfma_f32_16x16x32_bf16 v[56:59], a[68:71], v[72:75], v[56:59]\n"
"ds_read_b64_tr_b16 a[154:155], v12 offset:6144\n"
"v_mfma_f32_16x16x32_bf16 v[52:55], a[36:39], v[68:71], v[52:55]\n"
"ds_read_b128 v[108:111], v230 offset:8192\n"
"v_mfma_f32_16x16x32_bf16 v[52:55], a[40:43], v[128:131], v[52:55]\n"
"ds_read_b128 v[124:127], v230 offset:10240\n"
"v_mfma_f32_16x16x32_bf16 v[52:55], a[44:47], v[132:135], v[52:55]\n"
"ds_read_b128 v[80:83], v233 offset:8192\n"
"v_mfma_f32_16x16x32_bf16 v[52:55], a[48:51], v[92:95], v[52:55]\n"
"ds_read_b128 v[116:119], v231 offset:9216\n"
"v_mfma_f32_16x16x32_bf16 v[52:55], a[52:55], v[104:107], v[52:55]\n"
"ds_read_b128 v[84:87], v231 offset:11264\n"
"v_mfma_f32_16x16x32_bf16 v[52:55], a[56:59], v[64:67], v[52:55]\n"
"buffer_store_dwordx4 v[188:191], v1, s[52:55], s10 offen offset:1024 sc1\n"
"v_mfma_f32_16x16x32_bf16 v[52:55], a[60:63], v[88:91], v[52:55]\n"
"ds_read_b128 v[72:75], v234 offset:9216\n"
"v_mfma_f32_16x16x32_bf16 v[52:55], a[64:67], v[96:99], v[52:55]\n"
"ds_read_b128 v[88:91], v230 offset:9216\n"
"v_mfma_f32_16x16x32_bf16 v[52:55], a[68:71], v[100:103], v[52:55]\n"
"ds_read_b128 v[96:99], v230 offset:11264\n"
"v_mfma_f32_16x16x32_bf16 v[48:51], a[36:39], v[92:95], v[48:51]\n"
"ds_read_b128 v[100:103], v233 offset:9216\n"
"v_mfma_f32_16x16x32_bf16 v[48:51], a[40:43], v[104:107], v[48:51]\n"
"ds_read_b64_tr_b16 a[156:157], v12 offset:4104\n"
"v_mfma_f32_16x16x32_bf16 v[48:51], a[44:47], v[64:67], v[48:51]\n"
"ds_read_b64_tr_b16 a[158:159], v12 offset:6152\n"
"s_waitcnt lgkmcnt(4)\n"
"v_mfma_f32_16x16x32_bf16 v[48:51], a[48:51], v[112:115], v[48:51]\n"
"ds_read_b64_tr_b16 a[160:161], v12 offset:8192\n"
"v_mfma_f32_16x16x32_bf16 v[48:51], a[52:55], v[120:123], v[48:51]\n"
"buffer_store_dwordx4 v[188:191], v1, s[52:55], s19 offen offset:0 sc1\n"
"v_mfma_f32_16x16x32_bf16 v[48:51], a[56:59], v[76:79], v[48:51]\n"
"ds_read_b64_tr_b16 a[162:163], v12 offset:10240\n"
"v_mfma_f32_16x16x32_bf16 v[48:51], a[60:63], v[68:71], v[48:51]\n"
"ds_read_b128 v[92:95], v232 offset:8192\n"
"v_mfma_f32_16x16x32_bf16 v[48:51], a[64:67], v[128:131], v[48:51]\n"
"ds_read_b128 v[104:107], v232 offset:10240\n"
"v_mfma_f32_16x16x32_bf16 v[48:51], a[68:71], v[132:135], v[48:51]\n"
"ds_read_b128 v[64:67], v235 offset:8192\n"
"v_mfma_f32_16x16x32_bf16 v[48:51], a[72:75], v[108:111], v[48:51]\n"
"ds_read_b64_tr_b16 a[164:165], v12 offset:8200\n"
"v_mfma_f32_16x16x32_bf16 v[48:51], a[76:79], v[124:127], v[48:51]\n"
"ds_read_b64_tr_b16 a[166:167], v12 offset:10248\n"
"v_mfma_f32_16x16x32_bf16 v[48:51], a[80:83], v[80:83], v[48:51]\n"
"ds_read_b128 v[112:115], v233 offset:10240\n"
"v_mfma_f32_16x16x32_bf16 v[48:51], a[84:87], v[116:119], v[48:51]\n"
"buffer_store_dwordx4 v[188:191], v1, s[52:55], s19 offen offset:1024 sc1\n"
"v_mfma_f32_16x16x32_bf16 v[48:51], a[88:91], v[84:87], v[48:51]\n"
"ds_read_b64_tr_b16 a[168:169], v12 offset:12288\n"
"v_mfma_f32_16x16x32_bf16 v[48:51], a[92:95], v[72:75], v[48:51]\n"
"ds_read_b128 v[120:123], v234 offset:11264\n"
"v_mfma_f32_16x16x32_bf16 v[48:51], a[96:99], v[88:91], v[48:51]\n"
"buffer_load_dwordx4 v[240:243], v24, s[44:47], s14 offen\n"
"s_waitcnt lgkmcnt(2)\n"
"v_mfma_f32_16x16x32_bf16 v[48:51], a[100:103], v[96:99], v[48:51]\n"
"ds_read_b64_tr_b16 a[170:171], v12 offset:14336\n"
"v_mfma_f32_16x16x32_bf16 v[48:51], a[104:107], v[100:103], v[48:51]\n"
"ds_read_b128 v[76:79], v233 offset:11264\n"
"v_mfma_f32_16x16x32_bf16 v[52:55], a[72:75], v[88:91], v[52:55]\n"
"buffer_load_dwordx4 v[244:247], v24, s[44:47], s80 offen\n"
"v_mfma_f32_16x16x32_bf16 v[52:55], a[76:79], v[96:99], v[52:55]\n"
"ds_read_b64_tr_b16 a[172:173], v12 offset:12296\n"
"v_mfma_f32_16x16x32_bf16 v[52:55], a[80:83], v[100:103], v[52:55]\n"
"ds_read_b64_tr_b16 a[174:175], v12 offset:14344\n"
"v_mfma_f32_16x16x32_bf16 v[52:55], a[84:87], v[108:111], v[52:55]\n"
"v_add_u32_e32 v224, s34, v1\n"
"v_add_u32_e32 v225, s34, v19\n"
"v_mfma_f32_16x16x32_bf16 v[52:55], a[88:91], v[124:127], v[52:55]\n"
"v_add_u32_e32 v226, s34, v20\n"
"v_add_u32_e32 v227, s73, v1\n"
"v_mfma_f32_16x16x32_bf16 v[52:55], a[92:95], v[80:83], v[52:55]\n"
"v_add_u32_e32 v228, s73, v19\n"
"v_add_u32_e32 v229, s73, v20\n"
"v_mfma_f32_16x16x32_bf16 v[52:55], a[96:99], v[92:95], v[52:55]\n"
"ds_read_b64_tr_b16 a[176:177], v12 offset:16384\n"
"v_mfma_f32_16x16x32_bf16 v[52:55], a[100:103], v[104:107], v[52:55]\n"
"ds_read_b64_tr_b16 a[178:179], v12 offset:18432\n"
"v_mfma_f32_16x16x32_bf16 v[52:55], a[104:107], v[64:67], v[52:55]\n"
"ds_read_b128 v[68:71], v235 offset:10240\n"
"v_mfma_f32_16x16x32_bf16 v[56:59], a[72:75], v[124:127], v[56:59]\n"
"ds_read_b128 v[128:131], v230 offset:15360\n"
"v_mfma_f32_16x16x32_bf16 v[56:59], a[76:79], v[80:83], v[56:59]\n"
"ds_read_b128 v[132:135], v233 offset:13312\n"
"v_mfma_f32_16x16x32_bf16 v[56:59], a[80:83], v[112:115], v[56:59]\n"
"ds_read_b128 v[116:119], v233 offset:15360\n"
"v_mfma_f32_16x16x32_bf16 v[56:59], a[84:87], v[84:87], v[56:59]\n"
"ds_read_b128 v[88:91], v230 offset:14336\n"
"v_mfma_f32_16x16x32_bf16 v[56:59], a[88:91], v[72:75], v[56:59]\n"
"ds_read_b128 v[108:111], v233 offset:12288\n"
"s_waitcnt lgkmcnt(4)\n"
"v_mfma_f32_16x16x32_bf16 v[56:59], a[92:95], v[120:123], v[56:59]\n"
"ds_read_b128 v[92:95], v233 offset:14336\n"
"v_mfma_f32_16x16x32_bf16 v[56:59], a[96:99], v[96:99], v[56:59]\n"
"ds_read_b128 v[84:87], v232 offset:14336\n"
"v_mfma_f32_16x16x32_bf16 v[56:59], a[100:103], v[100:103], v[56:59]\n"
"ds_read_b128 v[72:75], v235 offset:12288\n"
"v_mfma_f32_16x16x32_bf16 v[56:59], a[104:107], v[76:79], v[56:59]\n"
"ds_read_b128 v[120:123], v235 offset:14336\n"
"v_mfma_f32_16x16x32_bf16 v[60:63], a[72:75], v[96:99], v[60:63]\n"
"ds_read_b64_tr_b16 a[180:181], v12 offset:16392\n"
"v_mfma_f32_16x16x32_bf16 v[60:63], a[76:79], v[100:103], v[60:63]\n"
"ds_read_b64_tr_b16 a[182:183], v12 offset:18440\n"
"v_mfma_f32_16x16x32_bf16 v[60:63], a[80:83], v[76:79], v[60:63]\n"
"ds_read_b64_tr_b16 a[184:185], v12 offset:20480\n"
"v_mfma_f32_16x16x32_bf16 v[60:63], a[84:87], v[124:127], v[60:63]\n"
"ds_read_b128 v[96:99], v231 offset:15360\n"
"v_mfma_f32_16x16x32_bf16 v[60:63], a[88:91], v[80:83], v[60:63]\n"
"ds_read_b128 v[100:103], v234 offset:13312\n"
"v_mfma_f32_16x16x32_bf16 v[60:63], a[92:95], v[112:115], v[60:63]\n"
"ds_read_b128 v[76:79], v234 offset:15360\n"
"v_mfma_f32_16x16x32_bf16 v[60:63], a[96:99], v[104:107], v[60:63]\n"
"ds_read_b64_tr_b16 a[186:187], v12 offset:22528\n"
"v_mfma_f32_16x16x32_bf16 v[60:63], a[100:103], v[64:67], v[60:63]\n"
"ds_read_b64_tr_b16 a[188:189], v12 offset:20488\n"
"v_mfma_f32_16x16x32_bf16 v[60:63], a[104:107], v[68:71], v[60:63]\n"
"ds_read_b64_tr_b16 a[190:191], v12 offset:22536\n"
"v_mfma_f32_16x16x32_bf16 v[60:63], a[108:111], v[128:131], v[60:63]\n"
"ds_read_b128 v[124:127], v230 offset:13312\n"
"s_waitcnt lgkmcnt(4)\n"
"v_mfma_f32_16x16x32_bf16 v[60:63], a[112:115], v[132:135], v[60:63]\n"
"ds_read_b64_tr_b16 a[192:193], v12 offset:24576\n"
"v_mfma_f32_16x16x32_bf16 v[60:63], a[116:119], v[116:119], v[60:63]\n"
"ds_read_b64_tr_b16 a[194:195], v12 offset:26624\n"
"v_mfma_f32_16x16x32_bf16 v[60:63], a[120:123], v[88:91], v[60:63]\n"
"ds_read_b128 v[80:83], v230 offset:12288\n"
"v_mfma_f32_16x16x32_bf16 v[60:63], a[124:127], v[108:111], v[60:63]\n"
"ds_read_b64_tr_b16 a[196:197], v12 offset:24584\n"
"v_mfma_f32_16x16x32_bf16 v[60:63], a[128:131], v[92:95], v[60:63]\n"
"ds_read_b64_tr_b16 a[198:199], v12 offset:26632\n"
"v_mfma_f32_16x16x32_bf16 v[60:63], a[132:135], v[84:87], v[60:63]\n"
"ds_read_b128 v[112:115], v232 offset:12288\n"
"v_mfma_f32_16x16x32_bf16 v[60:63], a[136:139], v[72:75], v[60:63]\n"
"ds_read_b64_tr_b16 a[200:201], v12 offset:28672\n"
"v_mfma_f32_16x16x32_bf16 v[60:63], a[140:143], v[120:123], v[60:63]\n"
"ds_read_b64_tr_b16 a[202:203], v12 offset:30720\n"
"v_mfma_f32_16x16x32_bf16 v[56:59], a[108:111], v[88:91], v[56:59]\n"
"ds_read_b64_tr_b16 a[204:205], v12 offset:28680\n"
"v_mfma_f32_16x16x32_bf16 v[56:59], a[112:115], v[108:111], v[56:59]\n"
"ds_read_b64_tr_b16 a[206:207], v12 offset:30728\n"
"v_mfma_f32_16x16x32_bf16 v[56:59], a[116:119], v[92:95], v[56:59]\n"
"ds_read_b64_tr_b16 a[208:209], v9 offset:0\n"
"v_mfma_f32_16x16x32_bf16 v[56:59], a[120:123], v[96:99], v[56:59]\n"
"ds_read_b128 v[104:107], v231 offset:13312\n"
"v_mfma_f32_16x16x32_bf16 v[56:59], a[124:127], v[100:103], v[56:59]\n"
"ds_read_b64_tr_b16 a[210:211], v9 offset:2048\n"
"v_mfma_f32_16x16x32_bf16 v[56:59], a[128:131], v[76:79], v[56:59]\n"
"ds_read_b64_tr_b16 a[212:213], v9 offset:8\n"
"v_mfma_f32_16x16x32_bf16 v[56:59], a[132:135], v[128:131], v[56:59]\n"
"ds_read_b64_tr_b16 a[214:215], v9 offset:2056\n"
"v_mfma_f32_16x16x32_bf16 v[56:59], a[136:139], v[132:135], v[56:59]\n"
"ds_read_b64_tr_b16 a[216:217], v9 offset:4096\n"
"v_mfma_f32_16x16x32_bf16 v[56:59], a[140:143], v[116:119], v[56:59]\n"
"ds_read_b64_tr_b16 a[218:219], v9 offset:6144\n"
"s_waitcnt lgkmcnt(4)\n"
"v_mfma_f32_16x16x32_bf16 v[52:55], a[108:111], v[124:127], v[52:55]\n"
"ds_read_b128 v[64:67], v224 offset:0\n"
"v_mfma_f32_16x16x32_bf16 v[52:55], a[112:115], v[128:131], v[52:55]\n"
"ds_read_b128 v[68:71], v224 offset:2048\n"
"v_mfma_f32_16x16x32_bf16 v[52:55], a[116:119], v[132:135], v[52:55]\n"
"ds_read_b128 v[120:123], v227 offset:0\n"
"v_mfma_f32_16x16x32_bf16 v[52:55], a[120:123], v[80:83], v[52:55]\n"
"ds_read_b128 v[92:95], v225 offset:1024\n"
"v_mfma_f32_16x16x32_bf16 v[52:55], a[124:127], v[88:91], v[52:55]\n"
"ds_read_b128 v[76:79], v225 offset:3072\n"
"v_mfma_f32_16x16x32_bf16 v[52:55], a[128:131], v[108:111], v[52:55]\n"
"ds_read_b128 v[116:119], v228 offset:1024\n"
"v_mfma_f32_16x16x32_bf16 v[52:55], a[132:135], v[112:115], v[52:55]\n"
"ds_read_b128 v[112:115], v224 offset:1024\n"
"v_mfma_f32_16x16x32_bf16 v[52:55], a[136:139], v[84:87], v[52:55]\n"
"ds_read_b128 v[84:87], v224 offset:3072\n"
"v_mfma_f32_16x16x32_bf16 v[52:55], a[140:143], v[72:75], v[52:55]\n"
"ds_read_b128 v[72:75], v227 offset:1024\n"
"v_mfma_f32_16x16x32_bf16 v[48:51], a[108:111], v[80:83], v[48:51]\n"
"ds_read_b64_tr_b16 a[220:221], v9 offset:4104\n"
"v_mfma_f32_16x16x32_bf16 v[48:51], a[112:115], v[88:91], v[48:51]\n"
"ds_read_b64_tr_b16 a[222:223], v9 offset:6152\n"
"v_mfma_f32_16x16x32_bf16 v[48:51], a[116:119], v[108:111], v[48:51]\n"
"ds_read_b64_tr_b16 a[224:225], v9 offset:8192\n"
"v_mfma_f32_16x16x32_bf16 v[48:51], a[120:123], v[104:107], v[48:51]\n"
"ds_read_b64_tr_b16 a[226:227], v9 offset:10240\n"
"v_mfma_f32_16x16x32_bf16 v[48:51], a[124:127], v[96:99], v[48:51]\n"
"ds_read_b64_tr_b16 a[228:229], v9 offset:8200\n"
"v_mfma_f32_16x16x32_bf16 v[48:51], a[128:131], v[100:103], v[48:51]\n"
"ds_read_b64_tr_b16 a[230:231], v9 offset:10248\n"
"v_mfma_f32_16x16x32_bf16 v[48:51], a[132:135], v[124:127], v[48:51]\n"
"ds_read_b128 v[80:83], v226 offset:0\n"
"v_mfma_f32_16x16x32_bf16 v[48:51], a[136:139], v[128:131], v[48:51]\n"
"ds_read_b128 v[88:91], v226 offset:2048\n"
"v_mfma_f32_16x16x32_bf16 v[48:51], a[140:143], v[132:135], v[48:51]\n"
"ds_read_b128 v[108:111], v229 offset:0\n"
"s_waitcnt lgkmcnt(4)\n"
"v_mfma_f32_16x16x32_bf16 v[32:35], a[0:3], v[64:67], 0\n"
"ds_read_b128 v[104:107], v224 offset:5120\n"
"v_mfma_f32_16x16x32_bf16 v[32:35], a[4:7], v[68:71], v[32:35]\n"
"ds_read_b128 v[96:99], v224 offset:7168\n"
"v_mfma_f32_16x16x32_bf16 v[32:35], a[8:11], v[120:123], v[32:35]\n"
"ds_read_b128 v[100:103], v227 offset:5120\n"
"v_mfma_f32_16x16x32_bf16 v[32:35], a[12:15], v[92:95], v[32:35]\n"
"ds_read_b128 v[124:127], v224 offset:4096\n"
"v_mfma_f32_16x16x32_bf16 v[32:35], a[16:19], v[76:79], v[32:35]\n"
"ds_read_b128 v[128:131], v224 offset:6144\n"
"v_mfma_f32_16x16x32_bf16 v[32:35], a[20:23], v[116:119], v[32:35]\n"
"ds_read_b128 v[132:135], v227 offset:4096\n"
"v_mfma_f32_16x16x32_bf16 v[32:35], a[24:27], v[112:115], v[32:35]\n"
"ds_read_b128 v[92:95], v226 offset:4096\n"
"v_mfma_f32_16x16x32_bf16 v[32:35], a[28:31], v[84:87], v[32:35]\n"
"ds_read_b128 v[76:79], v226 offset:6144\n"
"v_mfma_f32_16x16x32_bf16 v[32:35], a[32:35], v[72:75], v[32:35]\n"
"ds_read_b128 v[116:119], v229 offset:4096\n"
"v_mfma_f32_16x16x32_bf16 v[36:39], a[0:3], v[112:115], 0\n"
"ds_read_b64_tr_b16 a[232:233], v9 offset:12288\n"
"v_mfma_f32_16x16x32_bf16 v[36:39], a[4:7], v[84:87], v[36:39]\n"
"v_mov_b32_e32 v189, s66\n"
"v_mfma_f32_16x16x32_bf16 v[36:39], a[8:11], v[72:75], v[36:39]\n"
"s_waitcnt vmcnt(2)\n"
"buffer_store_dwordx4 v[188:191], v27, s[52:55], s20 offen sc1\n"
"v_mfma_f32_16x16x32_bf16 v[36:39], a[12:15], v[64:67], v[36:39]\n"
"ds_read_b128 v[112:115], v225 offset:5120\n"
"v_mfma_f32_16x16x32_bf16 v[36:39], a[16:19], v[68:71], v[36:39]\n"
"v_cvt_pk_bf16_f32 v192, v48, v49\n"
"v_cvt_pk_bf16_f32 v193, v50, v51\n"
"v_mfma_f32_16x16x32_bf16 v[36:39], a[20:23], v[120:123], v[36:39]\n"
"ds_read_b128 v[84:87], v225 offset:7168\n"
"s_waitcnt lgkmcnt(2)\n"
"v_mfma_f32_16x16x32_bf16 v[36:39], a[24:27], v[80:83], v[36:39]\n"
"v_cvt_pk_bf16_f32 v194, v52, v53\n"
"v_cvt_pk_bf16_f32 v195, v54, v55\n"
"v_mfma_f32_16x16x32_bf16 v[36:39], a[28:31], v[88:91], v[36:39]\n"
"ds_read_b128 v[72:75], v228 offset:5120\n"
"v_mfma_f32_16x16x32_bf16 v[36:39], a[32:35], v[108:111], v[36:39]\n"
"v_min_u32_e64 v192, -2, v192\n"
"v_min_u32_e64 v193, -2, v193\n"
"v_mfma_f32_16x16x32_bf16 v[36:39], a[36:39], v[104:107], v[36:39]\n"
"v_min_u32_e64 v194, -2, v194\n"
"v_min_u32_e64 v195, -2, v195\n"
"v_mfma_f32_16x16x32_bf16 v[36:39], a[40:43], v[96:99], v[36:39]\n"
"ds_read_b128 v[64:67], v224 offset:8192\n"
"v_mfma_f32_16x16x32_bf16 v[36:39], a[44:47], v[100:103], v[36:39]\n"
"ds_read_b128 v[68:71], v224 offset:10240\n"
"v_mfma_f32_16x16x32_bf16 v[36:39], a[48:51], v[124:127], v[36:39]\n"
"buffer_store_dwordx4 v[192:195], v1, s[52:55], s9 offen offset:0 sc1\n"
"v_mfma_f32_16x16x32_bf16 v[36:39], a[52:55], v[128:131], v[36:39]\n"
"ds_read_b128 v[120:123], v227 offset:8192\n"
"v_mfma_f32_16x16x32_bf16 v[36:39], a[56:59], v[132:135], v[36:39]\n"
"v_cvt_pk_bf16_f32 v196, v56, v57\n"
"v_cvt_pk_bf16_f32 v197, v58, v59\n"
"v_mfma_f32_16x16x32_bf16 v[36:39], a[60:63], v[92:95], v[36:39]\n"
"ds_read_b128 v[80:83], v225 offset:9216\n"
"v_mfma_f32_16x16x32_bf16 v[36:39], a[64:67], v[76:79], v[36:39]\n"
"v_cvt_pk_bf16_f32 v198, v60, v61\n"
"v_cvt_pk_bf16_f32 v199, v62, v63\n"
"v_mfma_f32_16x16x32_bf16 v[36:39], a[68:71], v[116:119], v[36:39]\n"
"ds_read_b128 v[88:91], v225 offset:11264\n"
"v_mfma_f32_16x16x32_bf16 v[32:35], a[36:39], v[124:127], v[32:35]\n"
"v_min_u32_e64 v196, -2, v196\n"
"v_min_u32_e64 v197, -2, v197\n"
"v_mfma_f32_16x16x32_bf16 v[32:35], a[40:43], v[128:131], v[32:35]\n"
"ds_read_b128 v[108:111], v228 offset:9216\n"
"v_mfma_f32_16x16x32_bf16 v[32:35], a[44:47], v[132:135], v[32:35]\n"
"v_min_u32_e64 v198, -2, v198\n"
"v_min_u32_e64 v199, -2, v199\n"
"s_waitcnt lgkmcnt(2)\n"
"v_mfma_f32_16x16x32_bf16 v[32:35], a[48:51], v[112:115], v[32:35]\n"
"ds_read_b128 v[92:95], v224 offset:9216\n"
"v_mfma_f32_16x16x32_bf16 v[32:35], a[52:55], v[84:87], v[32:35]\n"
"ds_read_b128 v[76:79], v224 offset:11264\n"
"v_mfma_f32_16x16x32_bf16 v[32:35], a[56:59], v[72:75], v[32:35]\n"
"ds_read_b128 v[116:119], v227 offset:9216\n"
"v_mfma_f32_16x16x32_bf16 v[32:35], a[60:63], v[104:107], v[32:35]\n"
"buffer_store_dwordx4 v[196:199], v1, s[52:55], s9 offen offset:1024 sc1\n"
"v_mfma_f32_16x16x32_bf16 v[32:35], a[64:67], v[96:99], v[32:35]\n"
"ds_read_b128 v[124:127], v226 offset:8192\n"
"v_mfma_f32_16x16x32_bf16 v[32:35], a[68:71], v[100:103], v[32:35]\n"
"ds_read_b128 v[128:131], v226 offset:10240\n"
"v_mfma_f32_16x16x32_bf16 v[32:35], a[72:75], v[64:67], v[32:35]\n"
"ds_read_b128 v[132:135], v229 offset:8192\n"
"v_mfma_f32_16x16x32_bf16 v[32:35], a[76:79], v[68:71], v[32:35]\n"
"ds_read_b128 v[112:115], v224 offset:13312\n"
"v_mfma_f32_16x16x32_bf16 v[32:35], a[80:83], v[120:123], v[32:35]\n"
"ds_read_b128 v[84:87], v224 offset:15360\n"
"v_mfma_f32_16x16x32_bf16 v[32:35], a[84:87], v[80:83], v[32:35]\n"
"ds_read_b128 v[72:75], v227 offset:13312\n"
"s_waitcnt lgkmcnt(4)\n"
"v_mfma_f32_16x16x32_bf16 v[32:35], a[88:91], v[88:91], v[32:35]\n"
"ds_read_b128 v[104:107], v224 offset:12288\n"
"v_mfma_f32_16x16x32_bf16 v[32:35], a[92:95], v[108:111], v[32:35]\n"
"ds_read_b128 v[96:99], v224 offset:14336\n"
"v_mfma_f32_16x16x32_bf16 v[32:35], a[96:99], v[92:95], v[32:35]\n"
"ds_read_b128 v[100:103], v227 offset:12288\n"
"v_mfma_f32_16x16x32_bf16 v[32:35], a[100:103], v[76:79], v[32:35]\n"
"ds_read_b128 v[80:83], v226 offset:12288\n"
"v_mfma_f32_16x16x32_bf16 v[32:35], a[104:107], v[116:119], v[32:35]\n"
"ds_read_b128 v[88:91], v226 offset:14336\n"
"v_mfma_f32_16x16x32_bf16 v[36:39], a[72:75], v[92:95], v[36:39]\n"
"ds_read_b128 v[108:111], v229 offset:12288\n"
"v_mfma_f32_16x16x32_bf16 v[36:39], a[76:79], v[76:79], v[36:39]\n"
"ds_read_b64_tr_b16 a[234:235], v9 offset:14336\n"
"v_mfma_f32_16x16x32_bf16 v[36:39], a[80:83], v[116:119], v[36:39]\n"
"ds_read_b64_tr_b16 a[236:237], v9 offset:12296\n"
"v_mfma_f32_16x16x32_bf16 v[36:39], a[84:87], v[64:67], v[36:39]\n"
"ds_read_b128 v[92:95], v225 offset:13312\n"
"v_mfma_f32_16x16x32_bf16 v[36:39], a[88:91], v[68:71], v[36:39]\n"
"ds_read_b128 v[76:79], v225 offset:15360\n"
"v_mfma_f32_16x16x32_bf16 v[36:39], a[92:95], v[120:123], v[36:39]\n"
"ds_read_b128 v[116:119], v228 offset:13312\n"
"v_mfma_f32_16x16x32_bf16 v[36:39], a[96:99], v[124:127], v[36:39]\n"
"ds_read_b64_tr_b16 a[238:239], v9 offset:14344\n"
"v_mfma_f32_16x16x32_bf16 v[36:39], a[100:103], v[128:131], v[36:39]\n"
"v_add_u32_e32 v230, s35, v1\n"
"v_add_u32_e32 v231, s35, v19\n"
"s_waitcnt lgkmcnt(3)\n"
"v_mfma_f32_16x16x32_bf16 v[36:39], a[104:107], v[132:135], v[36:39]\n"
"v_add_u32_e32 v232, s35, v20\n"
"v_add_u32_e32 v233, s74, v1\n"
"v_mfma_f32_16x16x32_bf16 v[36:39], a[108:111], v[112:115], v[36:39]\n"
"ds_read_b128 v[64:67], v224 offset:2048\n"
"v_mfma_f32_16x16x32_bf16 v[36:39], a[112:115], v[84:87], v[36:39]\n"
"ds_read_b128 v[68:71], v227 offset:0\n"
"v_mfma_f32_16x16x32_bf16 v[36:39], a[116:119], v[72:75], v[36:39]\n"
"ds_read_b128 v[120:123], v227 offset:2048\n"
"v_mfma_f32_16x16x32_bf16 v[36:39], a[120:123], v[104:107], v[36:39]\n"
"ds_read_b128 v[124:127], v225 offset:3072\n"
"v_mfma_f32_16x16x32_bf16 v[36:39], a[124:127], v[96:99], v[36:39]\n"
"ds_read_b128 v[128:131], v228 offset:1024\n"
"v_mfma_f32_16x16x32_bf16 v[36:39], a[128:131], v[100:103], v[36:39]\n"
"ds_read_b128 v[132:135], v228 offset:3072\n"
"v_mfma_f32_16x16x32_bf16 v[36:39], a[132:135], v[80:83], v[36:39]\n"
"ds_read_b128 v[80:83], v224 offset:3072\n"
"v_mfma_f32_16x16x32_bf16 v[36:39], a[136:139], v[88:91], v[36:39]\n"
"ds_read_b128 v[88:91], v227 offset:1024\n"
"v_mfma_f32_16x16x32_bf16 v[36:39], a[140:143], v[108:111], v[36:39]\n"
"ds_read_b128 v[108:111], v227 offset:3072\n"
"v_mfma_f32_16x16x32_bf16 v[32:35], a[108:111], v[104:107], v[32:35]\n"
"v_add_u32_e32 v234, s74, v19\n"
"v_add_u32_e32 v235, s74, v20\n"
"v_mfma_f32_16x16x32_bf16 v[32:35], a[112:115], v[96:99], v[32:35]\n"
"ds_read_b64_tr_b16 a[240:241], v9 offset:16384\n"
"v_mfma_f32_16x16x32_bf16 v[32:35], a[116:119], v[100:103], v[32:35]\n"
"ds_read_b64_tr_b16 a[242:243], v9 offset:18432\n"
"v_mfma_f32_16x16x32_bf16 v[32:35], a[120:123], v[92:95], v[32:35]\n"
"ds_read_b64_tr_b16 a[244:245], v9 offset:16392\n"
"s_waitcnt lgkmcnt(3)\n"
"v_mfma_f32_16x16x32_bf16 v[32:35], a[124:127], v[76:79], v[32:35]\n"
"ds_read_b64_tr_b16 a[246:247], v9 offset:18440\n"
"v_mfma_f32_16x16x32_bf16 v[32:35], a[128:131], v[116:119], v[32:35]\n"
"ds_read_b64_tr_b16 a[248:249], v9 offset:20480\n"
"v_mfma_f32_16x16x32_bf16 v[32:35], a[132:135], v[112:115], v[32:35]\n"
"ds_read_b128 v[104:107], v226 offset:2048\n"
"v_mfma_f32_16x16x32_bf16 v[32:35], a[136:139], v[84:87], v[32:35]\n"
"ds_read_b128 v[96:99], v229 offset:0\n"
"v_mfma_f32_16x16x32_bf16 v[32:35], a[140:143], v[72:75], v[32:35]\n"
"ds_read_b128 v[100:103], v229 offset:2048\n"
"v_mfma_f32_16x16x32_bf16 v[40:43], a[0:3], v[64:67], 0\n"
"ds_read_b128 v[92:95], v224 offset:7168\n"
"v_mfma_f32_16x16x32_bf16 v[40:43], a[4:7], v[68:71], v[40:43]\n"
"ds_read_b128 v[76:79], v227 offset:5120\n"
"v_mfma_f32_16x16x32_bf16 v[40:43], a[8:11], v[120:123], v[40:43]\n"
"ds_read_b128 v[116:119], v227 offset:7168\n"
"v_mfma_f32_16x16x32_bf16 v[40:43], a[12:15], v[124:127], v[40:43]\n"
"v_cvt_pk_bf16_f32 v192, v32, v33\n"
"v_cvt_pk_bf16_f32 v193, v34, v35\n"
"v_mfma_f32_16x16x32_bf16 v[40:43], a[16:19], v[128:131], v[40:43]\n"
"v_cvt_pk_bf16_f32 v194, v36, v37\n"
"v_cvt_pk_bf16_f32 v195, v38, v39\n"
"v_mfma_f32_16x16x32_bf16 v[40:43], a[20:23], v[132:135], v[40:43]\n"
"v_min_u32_e64 v192, -2, v192\n"
"v_min_u32_e64 v193, -2, v193\n"
"v_mfma_f32_16x16x32_bf16 v[40:43], a[24:27], v[80:83], v[40:43]\n"
"v_min_u32_e64 v194, -2, v194\n"
"v_min_u32_e64 v195, -2, v195\n"
"v_mfma_f32_16x16x32_bf16 v[40:43], a[28:31], v[88:91], v[40:43]\n"
"buffer_store_dwordx4 v[192:195], v1, s[52:55], s10 offen offset:0 sc1\n"
"v_mfma_f32_16x16x32_bf16 v[40:43], a[32:35], v[108:111], v[40:43]\n"
"ds_read_b128 v[112:115], v224 offset:6144\n"
"v_mfma_f32_16x16x32_bf16 v[44:47], a[0:3], v[80:83], 0\n"
"ds_read_b128 v[84:87], v227 offset:4096\n"
"v_mfma_f32_16x16x32_bf16 v[44:47], a[4:7], v[88:91], v[44:47]\n"
"ds_read_b128 v[72:75], v227 offset:6144\n"
"v_mfma_f32_16x16x32_bf16 v[44:47], a[8:11], v[108:111], v[44:47]\n"
"ds_read_b128 v[124:127], v226 offset:6144\n"
"v_mfma_f32_16x16x32_bf16 v[44:47], a[12:15], v[64:67], v[44:47]\n"
"ds_read_b128 v[128:131], v229 offset:4096\n"
"v_mfma_f32_16x16x32_bf16 v[44:47], a[16:19], v[68:71], v[44:47]\n"
"ds_read_b128 v[132:135], v229 offset:6144\n"
"v_mfma_f32_16x16x32_bf16 v[44:47], a[20:23], v[120:123], v[44:47]\n"
"ds_read_b128 v[80:83], v225 offset:7168\n"
"s_waitcnt lgkmcnt(4)\n"
"v_mfma_f32_16x16x32_bf16 v[44:47], a[24:27], v[104:107], v[44:47]\n"
"ds_read_b128 v[88:91], v228 offset:5120\n"
"v_mfma_f32_16x16x32_bf16 v[44:47], a[28:31], v[96:99], v[44:47]\n"
"ds_read_b128 v[108:111], v228 offset:7168\n"
"v_mfma_f32_16x16x32_bf16 v[44:47], a[32:35], v[100:103], v[44:47]\n"
"ds_read_b64_tr_b16 a[250:251], v9 offset:22528\n"
"v_mfma_f32_16x16x32_bf16 v[44:47], a[36:39], v[92:95], v[44:47]\n"
"ds_read_b128 v[64:67], v224 offset:10240\n"
"v_mfma_f32_16x16x32_bf16 v[44:47], a[40:43], v[76:79], v[44:47]\n"
"ds_read_b128 v[68:71], v227 offset:8192\n"
"v_mfma_f32_16x16x32_bf16 v[44:47], a[44:47], v[116:119], v[44:47]\n"
"ds_read_b128 v[120:123], v227 offset:10240\n"
"v_mfma_f32_16x16x32_bf16 v[44:47], a[48:51], v[112:115], v[44:47]\n"
"ds_read_b128 v[104:107], v225 offset:11264\n"
"v_mfma_f32_16x16x32_bf16 v[44:47], a[52:55], v[84:87], v[44:47]\n"
"ds_read_b128 v[96:99], v228 offset:9216\n"
"v_mfma_f32_16x16x32_bf16 v[44:47], a[56:59], v[72:75], v[44:47]\n"
"ds_read_b128 v[100:103], v228 offset:11264\n"
"s_waitcnt lgkmcnt(4)\n"
"v_mfma_f32_16x16x32_bf16 v[44:47], a[60:63], v[124:127], v[44:47]\n"
"ds_read_b128 v[124:127], v224 offset:11264\n"
"v_mfma_f32_16x16x32_bf16 v[44:47], a[64:67], v[128:131], v[44:47]\n"
"ds_read_b128 v[128:131], v227 offset:9216\n"
"v_mfma_f32_16x16x32_bf16 v[44:47], a[68:71], v[132:135], v[44:47]\n"
"ds_read_b128 v[132:135], v227 offset:11264\n"
"v_mfma_f32_16x16x32_bf16 v[40:43], a[36:39], v[112:115], v[40:43]\n"
"ds_read_b64_tr_b16 a[252:253], v9 offset:20488\n"
"v_mfma_f32_16x16x32_bf16 v[40:43], a[40:43], v[84:87], v[40:43]\n"
"ds_read_b64_tr_b16 a[254:255], v9 offset:22536\n"
"v_mfma_f32_16x16x32_bf16 v[40:43], a[44:47], v[72:75], v[40:43]\n"
"ds_read_b64_tr_b16 v[208:209], v9 offset:24576\n"
"v_mfma_f32_16x16x32_bf16 v[40:43], a[48:51], v[80:83], v[40:43]\n"
"ds_read_b64_tr_b16 v[210:211], v9 offset:26624\n"
"v_mfma_f32_16x16x32_bf16 v[40:43], a[52:55], v[88:91], v[40:43]\n"
"ds_read_b64_tr_b16 v[212:213], v9 offset:24584\n"
"v_mfma_f32_16x16x32_bf16 v[40:43], a[56:59], v[108:111], v[40:43]\n"
"ds_read_b64_tr_b16 v[214:215], v9 offset:26632\n"
"v_mfma_f32_16x16x32_bf16 v[40:43], a[60:63], v[92:95], v[40:43]\n"
"ds_read_b128 v[112:115], v226 offset:10240\n"
"v_mfma_f32_16x16x32_bf16 v[40:43], a[64:67], v[76:79], v[40:43]\n"
"ds_read_b128 v[84:87], v229 offset:8192\n"
"v_mfma_f32_16x16x32_bf16 v[40:43], a[68:71], v[116:119], v[40:43]\n"
"ds_read_b128 v[72:75], v229 offset:10240\n"
"v_mfma_f32_16x16x32_bf16 v[40:43], a[72:75], v[64:67], v[40:43]\n"
"ds_read_b128 v[80:83], v224 offset:15360\n"
"v_mfma_f32_16x16x32_bf16 v[40:43], a[76:79], v[68:71], v[40:43]\n"
"ds_read_b128 v[88:91], v227 offset:13312\n"
"s_waitcnt lgkmcnt(4)\n"
"v_mfma_f32_16x16x32_bf16 v[40:43], a[80:83], v[120:123], v[40:43]\n"
"buffer_load_dword v254, v17, s[52:55], s79 offen sc1\n"
"v_mfma_f32_16x16x32_bf16 v[40:43], a[84:87], v[104:107], v[40:43]\n"
"ds_read_b128 v[108:111], v227 offset:15360\n"
"v_mfma_f32_16x16x32_bf16 v[40:43], a[88:91], v[96:99], v[40:43]\n"
"ds_read_b128 v[92:95], v224 offset:14336\n"
"v_mfma_f32_16x16x32_bf16 v[40:43], a[92:95], v[100:103], v[40:43]\n"
"ds_read_b128 v[76:79], v227 offset:12288\n"
"v_mfma_f32_16x16x32_bf16 v[40:43], a[96:99], v[124:127], v[40:43]\n"
"ds_read_b128 v[116:119], v227 offset:14336\n"
"v_mfma_f32_16x16x32_bf16 v[40:43], a[100:103], v[128:131], v[40:43]\n"
"ds_read_b128 v[104:107], v226 offset:14336\n"
"v_mfma_f32_16x16x32_bf16 v[40:43], a[104:107], v[132:135], v[40:43]\n"
"ds_read_b128 v[96:99], v229 offset:12288\n"
"v_mfma_f32_16x16x32_bf16 v[44:47], a[72:75], v[124:127], v[44:47]\n"
"ds_read_b128 v[100:103], v229 offset:14336\n"
"v_mfma_f32_16x16x32_bf16 v[44:47], a[76:79], v[128:131], v[44:47]\n"
"ds_read_b64_tr_b16 v[216:217], v9 offset:28672\n"
"v_mfma_f32_16x16x32_bf16 v[44:47], a[80:83], v[132:135], v[44:47]\n"
"ds_read_b64_tr_b16 v[218:219], v9 offset:30720\n"
"v_mfma_f32_16x16x32_bf16 v[44:47], a[84:87], v[64:67], v[44:47]\n"
"ds_read_b64_tr_b16 v[220:221], v9 offset:28680\n"
"v_mfma_f32_16x16x32_bf16 v[44:47], a[88:91], v[68:71], v[44:47]\n"
"ds_read_b64_tr_b16 v[222:223], v9 offset:30728\n"
"v_mfma_f32_16x16x32_bf16 v[44:47], a[92:95], v[120:123], v[44:47]\n"
"ds_read_b128 v[124:127], v225 offset:15360\n"
"v_mfma_f32_16x16x32_bf16 v[44:47], a[96:99], v[112:115], v[44:47]\n"
"ds_read_b128 v[128:131], v228 offset:13312\n"
"s_waitcnt lgkmcnt(4)\n"
"v_mfma_f32_16x16x32_bf16 v[44:47], a[100:103], v[84:87], v[44:47]\n"
"ds_read_b128 v[132:135], v228 offset:15360\n"
"v_mfma_f32_16x16x32_bf16 v[44:47], a[104:107], v[72:75], v[44:47]\n"
"v_mfma_f32_16x16x32_bf16 v[44:47], a[108:111], v[80:83], v[44:47]\n"
"ds_read_b128 v[64:67], v230 offset:0\n"
"v_mfma_f32_16x16x32_bf16 v[44:47], a[112:115], v[88:91], v[44:47]\n"
"ds_read_b128 v[68:71], v230 offset:2048\n"
"v_mfma_f32_16x16x32_bf16 v[44:47], a[116:119], v[108:111], v[44:47]\n"
"ds_read_b128 v[120:123], v233 offset:0\n"
"v_mfma_f32_16x16x32_bf16 v[44:47], a[120:123], v[92:95], v[44:47]\n"
"ds_read_b128 v[112:115], v231 offset:1024\n"
"v_mfma_f32_16x16x32_bf16 v[44:47], a[124:127], v[76:79], v[44:47]\n"
"ds_read_b128 v[84:87], v231 offset:3072\n"
"v_mfma_f32_16x16x32_bf16 v[44:47], a[128:131], v[116:119], v[44:47]\n"
"ds_read_b128 v[72:75], v234 offset:1024\n"
"v_mfma_f32_16x16x32_bf16 v[44:47], a[132:135], v[104:107], v[44:47]\n"
"ds_read_b128 v[104:107], v230 offset:1024\n"
"v_mfma_f32_16x16x32_bf16 v[44:47], a[136:139], v[96:99], v[44:47]\n"
"ds_read_b128 v[96:99], v230 offset:3072\n"
"v_mfma_f32_16x16x32_bf16 v[44:47], a[140:143], v[100:103], v[44:47]\n"
"ds_read_b128 v[100:103], v233 offset:1024\n"
"v_mfma_f32_16x16x32_bf16 v[40:43], a[108:111], v[92:95], v[40:43]\n"
"s_waitcnt lgkmcnt(12)\n"
"s_barrier\n"
"v_mfma_f32_16x16x32_bf16 v[40:43], a[112:115], v[76:79], v[40:43]\n"
"v_mfma_f32_16x16x32_bf16 v[40:43], a[116:119], v[116:119], v[40:43]\n"
"s_waitcnt lgkmcnt(1)\n"
"v_mfma_f32_16x16x32_bf16 v[40:43], a[120:123], v[124:127], v[40:43]\n"
"v_mfma_f32_16x16x32_bf16 v[40:43], a[124:127], v[128:131], v[40:43]\n"
"v_mfma_f32_16x16x32_bf16 v[40:43], a[128:131], v[132:135], v[40:43]\n"
"v_mfma_f32_16x16x32_bf16 v[40:43], a[132:135], v[80:83], v[40:43]\n"
"ds_read_b128 v[92:95], v232 offset:0\n"
"v_mfma_f32_16x16x32_bf16 v[40:43], a[136:139], v[88:91], v[40:43]\n"
"ds_read_b128 v[76:79], v232 offset:2048\n"
"v_mfma_f32_16x16x32_bf16 v[40:43], a[140:143], v[108:111], v[40:43]\n"
"ds_read_b128 v[116:119], v235 offset:0\n"
"v_mfma_f32_16x16x32_bf16 v[48:51], a[0:3], v[64:67], v[144:147]\n"
"ds_read_b128 v[124:127], v230 offset:5120\n"
"v_mfma_f32_16x16x32_bf16 v[48:51], a[4:7], v[68:71], v[48:51]\n"
"ds_read_b128 v[128:131], v230 offset:7168\n"
"v_mfma_f32_16x16x32_bf16 v[48:51], a[8:11], v[120:123], v[48:51]\n"
"ds_read_b128 v[132:135], v233 offset:5120\n"
"v_mfma_f32_16x16x32_bf16 v[48:51], a[12:15], v[112:115], v[48:51]\n"
"v_cvt_pk_bf16_f32 v196, v40, v41\n"
"v_cvt_pk_bf16_f32 v197, v42, v43\n"
"v_mfma_f32_16x16x32_bf16 v[48:51], a[16:19], v[84:87], v[48:51]\n"
"v_cvt_pk_bf16_f32 v198, v44, v45\n"
"v_cvt_pk_bf16_f32 v199, v46, v47\n"
"v_mfma_f32_16x16x32_bf16 v[48:51], a[20:23], v[72:75], v[48:51]\n"
"v_min_u32_e64 v196, -2, v196\n"
"v_min_u32_e64 v197, -2, v197\n"
"v_mfma_f32_16x16x32_bf16 v[48:51], a[24:27], v[104:107], v[48:51]\n"
"v_min_u32_e64 v198, -2, v198\n"
"v_min_u32_e64 v199, -2, v199\n"
"v_mfma_f32_16x16x32_bf16 v[48:51], a[28:31], v[96:99], v[48:51]\n"
"buffer_store_dwordx4 v[196:199], v1, s[52:55], s10 offen offset:1024 sc1\n"
"s_waitcnt lgkmcnt(0)\n"
"v_mfma_f32_16x16x32_bf16 v[48:51], a[32:35], v[100:103], v[48:51]\n"
"ds_read_b128 v[80:83], v230 offset:4096\n"
"v_mfma_f32_16x16x32_bf16 v[52:55], a[0:3], v[104:107], v[148:151]\n"
"ds_read_b128 v[88:91], v230 offset:6144\n"
"v_mfma_f32_16x16x32_bf16 v[52:55], a[4:7], v[96:99], v[52:55]\n"
"ds_read_b128 v[108:111], v233 offset:4096\n"
"v_mfma_f32_16x16x32_bf16 v[52:55], a[8:11], v[100:103], v[52:55]\n"
"ds_read_b128 v[112:115], v232 offset:4096\n"
"v_mfma_f32_16x16x32_bf16 v[52:55], a[12:15], v[64:67], v[52:55]\n"
"ds_read_b128 v[84:87], v232 offset:6144\n"
"v_mfma_f32_16x16x32_bf16 v[52:55], a[16:19], v[68:71], v[52:55]\n"
"ds_read_b128 v[72:75], v235 offset:4096\n"
"v_mfma_f32_16x16x32_bf16 v[52:55], a[20:23], v[120:123], v[52:55]\n"
"ds_read_b128 v[104:107], v231 offset:5120\n"
"v_mfma_f32_16x16x32_bf16 v[52:55], a[24:27], v[92:95], v[52:55]\n"
"ds_read_b128 v[96:99], v231 offset:7168\n"
"v_mfma_f32_16x16x32_bf16 v[52:55], a[28:31], v[76:79], v[52:55]\n"
"ds_read_b128 v[100:103], v234 offset:5120\n"
"v_mfma_f32_16x16x32_bf16 v[52:55], a[32:35], v[116:119], v[52:55]\n"
"s_waitcnt vmcnt(1)\n"
"v_cmp_ne_u32_e32 vcc, s66, v254\n"
"s_and_b64 vcc, vcc, 0x7ff00000\n"
"s_cbranch_vccnz .Lcn_R_t2\n"
".Lcn_L_t2ok:\n"
"v_mfma_f32_16x16x32_bf16 v[52:55], a[36:39], v[124:127], v[52:55]\n"
"ds_read_b128 v[64:67], v230 offset:8192\n"
"v_mfma_f32_16x16x32_bf16 v[52:55], a[40:43], v[128:131], v[52:55]\n"
"ds_read_b128 v[68:71], v230 offset:10240\n"
"v_mfma_f32_16x16x32_bf16 v[52:55], a[44:47], v[132:135], v[52:55]\n"
"s_add_u32 s76, s11, 0\n"
"s_waitcnt lgkmcnt(2)\n"
"v_mfma_f32_16x16x32_bf16 v[52:55], a[48:51], v[80:83], v[52:55]\n"
"buffer_load_dwordx4 v[160:163], v1, s[52:55], s76 offen offset:0 sc1\n"
"v_mfma_f32_16x16x32_bf16 v[52:55], a[52:55], v[88:91], v[52:55]\n"
"s_add_u32 s76, s11, 2097152\n"
"v_mfma_f32_16x16x32_bf16 v[52:55], a[56:59], v[108:111], v[52:55]\n"
"buffer_load_dwordx4 v[164:167], v1, s[52:55], s76 offen offset:0 sc1\n"
"v_mfma_f32_16x16x32_bf16 v[52:55], a[60:63], v[112:115], v[52:55]\n"
"s_add_u32 s76, s11, 4194304\n"
"v_mfma_f32_16x16x32_bf16 v[52:55], a[64:67], v[84:87], v[52:55]\n"
"buffer_load_dwordx4 v[168:171], v1, s[52:55], s76 offen offset:0 sc1\n"
"v_mfma_f32_16x16x32_bf16 v[52:55], a[68:71], v[72:75], v[52:55]\n"
"ds_read_b128 v[120:123], v233 offset:8192\n"
"v_mfma_f32_16x16x32_bf16 v[48:51], a[36:39], v[80:83], v[48:51]\n"
"ds_read_b128 v[92:95], v231 offset:9216\n"
"v_mfma_f32_16x16x32_bf16 v[48:51], a[40:43], v[88:91], v[48:51]\n"
"ds_read_b128 v[76:79], v231 offset:11264\n"
"v_mfma_f32_16x16x32_bf16 v[48:51], a[44:47], v[108:111], v[48:51]\n"
"ds_read_b128 v[116:119], v234 offset:9216\n"
"v_mfma_f32_16x16x32_bf16 v[48:51], a[48:51], v[104:107], v[48:51]\n"
"ds_read_b128 v[112:115], v230 offset:9216\n"
"v_mfma_f32_16x16x32_bf16 v[48:51], a[52:55], v[96:99], v[48:51]\n"
"ds_read_b128 v[84:87], v230 offset:11264\n"
"v_mfma_f32_16x16x32_bf16 v[48:51], a[56:59], v[100:103], v[48:51]\n"
"ds_read_b128 v[72:75], v233 offset:9216\n"
"v_mfma_f32_16x16x32_bf16 v[48:51], a[60:63], v[124:127], v[48:51]\n"
"ds_read_b128 v[80:83], v232 offset:8192\n"
"v_mfma_f32_16x16x32_bf16 v[48:51], a[64:67], v[128:131], v[48:51]\n"
"ds_read_b128 v[88:91], v232 offset:10240\n"
"v_mfma_f32_16x16x32_bf16 v[48:51], a[68:71], v[132:135], v[48:51]\n"
"ds_read_b128 v[108:111], v235 offset:8192\n"
"s_waitcnt lgkmcnt(4)\n"
"v_mfma_f32_16x16x32_bf16 v[48:51], a[72:75], v[64:67], v[48:51]\n"
"ds_read_b128 v[104:107], v230 offset:13312\n"
"v_mfma_f32_16x16x32_bf16 v[48:51], a[76:79], v[68:71], v[48:51]\n"
"ds_read_b128 v[96:99], v230 offset:15360\n"
"v_mfma_f32_16x16x32_bf16 v[48:51], a[80:83], v[120:123], v[48:51]\n"
"ds_read_b128 v[100:103], v233 offset:13312\n"
"v_mfma_f32_16x16x32_bf16 v[48:51], a[84:87], v[92:95], v[48:51]\n"
"ds_read_b128 v[124:127], v230 offset:12288\n"
"v_mfma_f32_16x16x32_bf16 v[48:51], a[88:91], v[76:79], v[48:51]\n"
"ds_read_b128 v[128:131], v230 offset:14336\n"
"v_mfma_f32_16x16x32_bf16 v[48:51], a[92:95], v[116:119], v[48:51]\n"
"ds_read_b128 v[132:135], v233 offset:12288\n"
"v_mfma_f32_16x16x32_bf16 v[48:51], a[96:99], v[112:115], v[48:51]\n"
"ds_read_b128 v[92:95], v232 offset:12288\n"
"v_mfma_f32_16x16x32_bf16 v[48:51], a[100:103], v[84:87], v[48:51]\n"
"ds_read_b128 v[76:79], v232 offset:14336\n"
"s_waitcnt lgkmcnt(4)\n"
"v_mfma_f32_16x16x32_bf16 v[48:51], a[104:107], v[72:75], v[48:51]\n"
"ds_read_b128 v[116:119], v235 offset:12288\n"
"v_mfma_f32_16x16x32_bf16 v[52:55], a[72:75], v[112:115], v[52:55]\n"
"v_mfma_f32_16x16x32_bf16 v[52:55], a[76:79], v[84:87], v[52:55]\n"
"v_mfma_f32_16x16x32_bf16 v[52:55], a[80:83], v[72:75], v[52:55]\n"
"v_mfma_f32_16x16x32_bf16 v[52:55], a[84:87], v[64:67], v[52:55]\n"
"ds_read_b128 v[112:115], v231 offset:13312\n"
"v_mfma_f32_16x16x32_bf16 v[52:55], a[88:91], v[68:71], v[52:55]\n"
"ds_read_b128 v[84:87], v231 offset:15360\n"
"v_mfma_f32_16x16x32_bf16 v[52:55], a[92:95], v[120:123], v[52:55]\n"
"ds_read_b128 v[72:75], v234 offset:13312\n"
"v_mfma_f32_16x16x32_bf16 v[52:55], a[96:99], v[80:83], v[52:55]\n"
"v_mfma_f32_16x16x32_bf16 v[52:55], a[100:103], v[88:91], v[52:55]\n"
"v_mfma_f32_16x16x32_bf16 v[52:55], a[104:107], v[108:111], v[52:55]\n"
"v_mfma_f32_16x16x32_bf16 v[52:55], a[108:111], v[104:107], v[52:55]\n"
"ds_read_b128 v[64:67], v230 offset:2048\n"
"v_mfma_f32_16x16x32_bf16 v[52:55], a[112:115], v[96:99], v[52:55]\n"
"ds_read_b128 v[68:71], v233 offset:0\n"
"v_mfma_f32_16x16x32_bf16 v[52:55], a[116:119], v[100:103], v[52:55]\n"
"ds_read_b128 v[120:123], v233 offset:2048\n"
"v_mfma_f32_16x16x32_bf16 v[52:55], a[120:123], v[124:127], v[52:55]\n"
"ds_read_b128 v[80:83], v231 offset:3072\n"
"s_waitcnt lgkmcnt(4)\n"
"v_mfma_f32_16x16x32_bf16 v[52:55], a[124:127], v[128:131], v[52:55]\n"
"ds_read_b128 v[88:91], v234 offset:1024\n"
"v_mfma_f32_16x16x32_bf16 v[52:55], a[128:131], v[132:135], v[52:55]\n"
"ds_read_b128 v[108:111], v234 offset:3072\n"
"v_mfma_f32_16x16x32_bf16 v[52:55], a[132:135], v[92:95], v[52:55]\n"
"ds_read_b128 v[92:95], v230 offset:3072\n"
"v_mfma_f32_16x16x32_bf16 v[52:55], a[136:139], v[76:79], v[52:55]\n"
"ds_read_b128 v[76:79], v233 offset:1024\n"
"v_mfma_f32_16x16x32_bf16 v[52:55], a[140:143], v[116:119], v[52:55]\n"
"ds_read_b128 v[116:119], v233 offset:3072\n"
"v_mfma_f32_16x16x32_bf16 v[48:51], a[108:111], v[124:127], v[48:51]\n"
"v_mfma_f32_16x16x32_bf16 v[48:51], a[112:115], v[128:131], v[48:51]\n"
"v_mfma_f32_16x16x32_bf16 v[48:51], a[116:119], v[132:135], v[48:51]\n"
"v_mfma_f32_16x16x32_bf16 v[48:51], a[120:123], v[112:115], v[48:51]\n"
"v_mfma_f32_16x16x32_bf16 v[48:51], a[124:127], v[84:87], v[48:51]\n"
"v_mfma_f32_16x16x32_bf16 v[48:51], a[128:131], v[72:75], v[48:51]\n"
"v_mfma_f32_16x16x32_bf16 v[48:51], a[132:135], v[104:107], v[48:51]\n"
"ds_read_b128 v[124:127], v232 offset:2048\n"
"v_mfma_f32_16x16x32_bf16 v[48:51], a[136:139], v[96:99], v[48:51]\n"
"ds_read_b128 v[128:131], v235 offset:0\n"
"v_mfma_f32_16x16x32_bf16 v[48:51], a[140:143], v[100:103], v[48:51]\n"
"ds_read_b128 v[132:135], v235 offset:2048\n"
"s_waitcnt vmcnt(0)\n"
"v_max3_u32 v248, v164, v165, v166\n"
"v_max3_u32 v249, v167, v168, v169\n"
"v_mfma_f32_16x16x32_bf16 v[48:51], v[202:205], v[160:163], v[48:51]\n"
"v_max3_u32 v248, v248, v170, v171\n"
"v_max_u32_e64 v248, v248, v249\n"
"v_mfma_f32_16x16x32_bf16 v[52:55], v[200:203], v[160:163], v[52:55]\n"
"v_cmp_eq_u32_e64 vcc, -1, v248\n"
"s_cbranch_vccnz .Lcn_R_x0\n"
".Lcn_L_x0ok:\n"
"v_mfma_f32_16x16x32_bf16 v[52:55], v[200:203], v[164:167], v[52:55]\n"
"v_mfma_f32_16x16x32_bf16 v[52:55], v[200:203], v[168:171], v[52:55]\n"
"v_mfma_f32_16x16x32_bf16 v[48:51], v[202:205], v[164:167], v[48:51]\n"
"v_mfma_f32_16x16x32_bf16 v[48:51], v[202:205], v[168:171], v[48:51]\n"
"s_waitcnt lgkmcnt(0)\n"
"v_mfma_f32_16x16x32_bf16 v[56:59], a[0:3], v[64:67], v[152:155]\n"
"ds_read_b128 v[112:115], v230 offset:7168\n"
"v_mfma_f32_16x16x32_bf16 v[56:59], a[4:7], v[68:71], v[56:59]\n"
"ds_read_b128 v[84:87], v233 offset:5120\n"
"v_mfma_f32_16x16x32_bf16 v[56:59], a[8:11], v[120:123], v[56:59]\n"
"ds_read_b128 v[72:75], v233 offset:7168\n"
"v_mfma_f32_16x16x32_bf16 v[56:59], a[12:15], v[80:83], v[56:59]\n"
"v_cvt_pk_bf16_f32 v136, v48, v49\n"
"v_cvt_pk_bf16_f32 v137, v50, v51\n"
"v_mfma_f32_16x16x32_bf16 v[56:59], a[16:19], v[88:91], v[56:59]\n"
"v_cvt_pk_bf16_f32 v138, v52, v53\n"
"v_cvt_pk_bf16_f32 v139, v54, v55\n"
"v_mfma_f32_16x16x32_bf16 v[56:59], a[20:23], v[108:111], v[56:59]\n"
"v_min_u32_e64 v136, -2, v136\n"
"v_min_u32_e64 v137, -2, v137\n"
"v_mfma_f32_16x16x32_bf16 v[56:59], a[24:27], v[92:95], v[56:59]\n"
"v_min_u32_e64 v138, -2, v138\n"
"v_min_u32_e64 v139, -2, v139\n"
"v_mfma_f32_16x16x32_bf16 v[56:59], a[28:31], v[76:79], v[56:59]\n"
"v_permlane16_swap_b32 v136, v138\n"
"v_permlane16_swap_b32 v137, v139\n"
"v_mfma_f32_16x16x32_bf16 v[56:59], a[32:35], v[116:119], v[56:59]\n"
"buffer_store_dwordx4 v[136:139], v23, s[52:55], s15 offen sc1\n"
"v_mfma_f32_16x16x32_bf16 v[60:63], a[0:3], v[92:95], v[156:159]\n"
"ds_read_b128 v[104:107], v230 offset:6144\n"
"v_mfma_f32_16x16x32_bf16 v[60:63], a[4:7], v[76:79], v[60:63]\n"
"ds_read_b128 v[96:99], v233 offset:4096\n"
"v_mfma_f32_16x16x32_bf16 v[60:63], a[8:11], v[116:119], v[60:63]\n"
"ds_read_b128 v[100:103], v233 offset:6144\n"
"v_mfma_f32_16x16x32_bf16 v[60:63], a[12:15], v[64:67], v[60:63]\n"
"ds_read_b128 v[80:83], v232 offset:6144\n"
"v_mfma_f32_16x16x32_bf16 v[60:63], a[16:19], v[68:71], v[60:63]\n"
"ds_read_b128 v[88:91], v235 offset:4096\n"
"v_mfma_f32_16x16x32_bf16 v[60:63], a[20:23], v[120:123], v[60:63]\n"
"ds_read_b128 v[108:111], v235 offset:6144\n"
"v_mfma_f32_16x16x32_bf16 v[60:63], a[24:27], v[124:127], v[60:63]\n"
"ds_read_b128 v[92:95], v231 offset:7168\n"
"v_mfma_f32_16x16x32_bf16 v[60:63], a[28:31], v[128:131], v[60:63]\n"
"ds_read_b128 v[76:79], v234 offset:5120\n"
"v_mfma_f32_16x16x32_bf16 v[60:63], a[32:35], v[132:135], v[60:63]\n"
"ds_read_b128 v[116:119], v234 offset:7168\n"
"s_waitcnt lgkmcnt(4)\n"
"v_mfma_f32_16x16x32_bf16 v[60:63], a[36:39], v[112:115], v[60:63]\n"
"ds_read_b128 v[64:67], v230 offset:10240\n"
"v_mfma_f32_16x16x32_bf16 v[60:63], a[40:43], v[84:87], v[60:63]\n"
"ds_read_b128 v[68:71], v233 offset:8192\n"
"v_mfma_f32_16x16x32_bf16 v[60:63], a[44:47], v[72:75], v[60:63]\n"
"ds_read_b128 v[120:123], v233 offset:10240\n"
"v_mfma_f32_16x16x32_bf16 v[60:63], a[48:51], v[104:107], v[60:63]\n"
"ds_read_b128 v[124:127], v231 offset:11264\n"
"v_mfma_f32_16x16x32_bf16 v[60:63], a[52:55], v[96:99], v[60:63]\n"
"ds_read_b128 v[128:131], v234 offset:9216\n"
"v_mfma_f32_16x16x32_bf16 v[60:63], a[56:59], v[100:103], v[60:63]\n"
"ds_read_b128 v[132:135], v234 offset:11264\n"
"v_mfma_f32_16x16x32_bf16 v[60:63], a[60:63], v[80:83], v[60:63]\n"
"s_add_u32 s76, s11, 0\n"
"v_mfma_f32_16x16x32_bf16 v[60:63], a[64:67], v[88:91], v[60:63]\n"
"buffer_load_dwordx4 v[172:175], v1, s[52:55], s76 offen offset:1024 sc1\n"
"s_waitcnt lgkmcnt(2)\n"
"v_mfma_f32_16x16x32_bf16 v[60:63], a[68:71], v[108:111], v[60:63]\n"
"s_add_u32 s76, s11, 2097152\n"
"v_mfma_f32_16x16x32_bf16 v[56:59], a[36:39], v[104:107], v[56:59]\n"
"buffer_load_dwordx4 v[176:179], v1, s[52:55], s76 offen offset:1024 sc1\n"
"v_mfma_f32_16x16x32_bf16 v[56:59], a[40:43], v[96:99], v[56:59]\n"
"s_add_u32 s76, s11, 4194304\n"
"v_mfma_f32_16x16x32_bf16 v[56:59], a[44:47], v[100:103], v[56:59]\n"
"buffer_load_dwordx4 v[180:183], v1, s[52:55], s76 offen offset:1024 sc1\n"
"v_mfma_f32_16x16x32_bf16 v[56:59], a[48:51], v[92:95], v[56:59]\n"
"ds_read_b128 v[80:83], v230 offset:11264\n"
"v_mfma_f32_16x16x32_bf16 v[56:59], a[52:55], v[76:79], v[56:59]\n"
"ds_read_b128 v[88:91], v233 offset:9216\n"
"v_mfma_f32_16x16x32_bf16 v[56:59], a[56:59], v[116:119], v[56:59]\n"
"ds_read_b128 v[108:111], v233 offset:11264\n"
"v_mfma_f32_16x16x32_bf16 v[56:59], a[60:63], v[112:115], v[56:59]\n"
"ds_read_b128 v[104:107], v232 offset:10240\n"
"v_mfma_f32_16x16x32_bf16 v[56:59], a[64:67], v[84:87], v[56:59]\n"
"ds_read_b128 v[96:99], v235 offset:8192\n"
"v_mfma_f32_16x16x32_bf16 v[56:59], a[68:71], v[72:75], v[56:59]\n"
"ds_read_b128 v[100:103], v235 offset:10240\n"
"v_mfma_f32_16x16x32_bf16 v[56:59], a[72:75], v[64:67], v[56:59]\n"
"ds_read_b128 v[92:95], v230 offset:15360\n"
"v_mfma_f32_16x16x32_bf16 v[56:59], a[76:79], v[68:71], v[56:59]\n"
"ds_read_b128 v[76:79], v233 offset:13312\n"
"v_mfma_f32_16x16x32_bf16 v[56:59], a[80:83], v[120:123], v[56:59]\n"
"ds_read_b128 v[116:119], v233 offset:15360\n"
"v_mfma_f32_16x16x32_bf16 v[56:59], a[84:87], v[124:127], v[56:59]\n"
"ds_read_b128 v[112:115], v230 offset:14336\n"
"s_waitcnt lgkmcnt(4)\n"
"v_mfma_f32_16x16x32_bf16 v[56:59], a[88:91], v[128:131], v[56:59]\n"
"ds_read_b128 v[84:87], v233 offset:12288\n"
"v_mfma_f32_16x16x32_bf16 v[56:59], a[92:95], v[132:135], v[56:59]\n"
"ds_read_b128 v[72:75], v233 offset:14336\n"
"v_mfma_f32_16x16x32_bf16 v[56:59], a[96:99], v[80:83], v[56:59]\n"
"ds_read_b128 v[124:127], v232 offset:14336\n"
"v_mfma_f32_16x16x32_bf16 v[56:59], a[100:103], v[88:91], v[56:59]\n"
"ds_read_b128 v[128:131], v235 offset:12288\n"
"v_mfma_f32_16x16x32_bf16 v[56:59], a[104:107], v[108:111], v[56:59]\n"
"ds_read_b128 v[132:135], v235 offset:14336\n"
"v_mfma_f32_16x16x32_bf16 v[60:63], a[72:75], v[80:83], v[60:63]\n"
"v_mfma_f32_16x16x32_bf16 v[60:63], a[76:79], v[88:91], v[60:63]\n"
"v_mfma_f32_16x16x32_bf16 v[60:63], a[80:83], v[108:111], v[60:63]\n"
"v_mfma_f32_16x16x32_bf16 v[60:63], a[84:87], v[64:67], v[60:63]\n"
"ds_read_b128 v[80:83], v231 offset:15360\n"
"v_mfma_f32_16x16x32_bf16 v[60:63], a[88:91], v[68:71], v[60:63]\n"
"ds_read_b128 v[88:91], v234 offset:13312\n"
"v_mfma_f32_16x16x32_bf16 v[60:63], a[92:95], v[120:123], v[60:63]\n"
"ds_read_b128 v[108:111], v234 offset:15360\n"
"v_mfma_f32_16x16x32_bf16 v[60:63], a[96:99], v[104:107], v[60:63]\n"
"v_mfma_f32_16x16x32_bf16 v[60:63], a[100:103], v[96:99], v[60:63]\n"
"v_mfma_f32_16x16x32_bf16 v[60:63], a[104:107], v[100:103], v[60:63]\n"
"s_waitcnt lgkmcnt(1)\n"
"v_mfma_f32_16x16x32_bf16 v[60:63], a[108:111], v[92:95], v[60:63]\n"
"v_mfma_f32_16x16x32_bf16 v[60:63], a[112:115], v[76:79], v[60:63]\n"
"v_mfma_f32_16x16x32_bf16 v[60:63], a[116:119], v[116:119], v[60:63]\n"
"v_mfma_f32_16x16x32_bf16 v[60:63], a[120:123], v[112:115], v[60:63]\n"
"v_mfma_f32_16x16x32_bf16 v[60:63], a[124:127], v[84:87], v[60:63]\n"
"v_mfma_f32_16x16x32_bf16 v[60:63], a[128:131], v[72:75], v[60:63]\n"
"v_mfma_f32_16x16x32_bf16 v[60:63], a[132:135], v[124:127], v[60:63]\n"
"v_mfma_f32_16x16x32_bf16 v[60:63], a[136:139], v[128:131], v[60:63]\n"
"v_mfma_f32_16x16x32_bf16 v[60:63], a[140:143], v[132:135], v[60:63]\n"
"v_mfma_f32_16x16x32_bf16 v[56:59], a[108:111], v[112:115], v[56:59]\n"
"v_mfma_f32_16x16x32_bf16 v[56:59], a[112:115], v[84:87], v[56:59]\n"
"v_mfma_f32_16x16x32_bf16 v[56:59], a[116:119], v[72:75], v[56:59]\n"
"v_mfma_f32_16x16x32_bf16 v[56:59], a[120:123], v[80:83], v[56:59]\n"
"s_add_u32 m0, s13, 81920\n"
"s_add_u32 s76, s17, 0\n"
"v_mfma_f32_16x16x32_bf16 v[56:59], a[124:127], v[88:91], v[56:59]\n"
"buffer_load_dwordx4 v1, s[52:55], s76 offen offset:0 sc1 lds\n"
"s_waitcnt lgkmcnt(0)\n"
"v_mfma_f32_16x16x32_bf16 v[56:59], a[128:131], v[108:111], v[56:59]\n"
"buffer_load_dwordx4 v1, s[52:55], s76 offen offset:1024 sc1 lds\n"
"v_mfma_f32_16x16x32_bf16 v[56:59], a[132:135], v[92:95], v[56:59]\n"
"buffer_load_dwordx4 v1, s[52:55], s76 offen offset:2048 sc1 lds\n"
"v_mfma_f32_16x16x32_bf16 v[56:59], a[136:139], v[76:79], v[56:59]\n"
"buffer_load_dwordx4 v1, s[52:55], s76 offen offset:3072 sc1 lds\n"
"v_mfma_f32_16x16x32_bf16 v[56:59], a[140:143], v[116:119], v[56:59]\n"
"s_add_u32 m0, s13, 98304\n"
"s_add_u32 s76, s17, 16384\n"
"s_waitcnt vmcnt(4)\n"
"v_max3_u32 v248, v176, v177, v178\n"
"v_max3_u32 v249, v179, v180, v181\n"
"v_mfma_f32_16x16x32_bf16 v[56:59], v[202:205], v[172:175], v[56:59]\n"
"v_max3_u32 v248, v248, v182, v183\n"
"v_max_u32_e64 v248, v248, v249\n"
"v_mfma_f32_16x16x32_bf16 v[60:63], v[200:203], v[172:175], v[60:63]\n"
"v_cmp_eq_u32_e64 vcc, -1, v248\n"
"s_cbranch_vccnz .Lcn_R_x1\n"
".Lcn_L_x1ok:\n"
"v_mfma_f32_16x16x32_bf16 v[60:63], v[200:203], v[176:179], v[60:63]\n"
"v_mfma_f32_16x16x32_bf16 v[60:63], v[200:203], v[180:183], v[60:63]\n"
"v_mfma_f32_16x16x32_bf16 v[56:59], v[202:205], v[176:179], v[56:59]\n"
"v_mfma_f32_16x16x32_bf16 v[56:59], v[202:205], v[180:183], v[56:59]\n"
"buffer_load_dwordx4 v1, s[52:55], s76 offen offset:0 sc1 lds\n"
"buffer_load_dwordx4 v1, s[52:55], s76 offen offset:1024 sc1 lds\n"
"buffer_load_dwordx4 v1, s[52:55], s76 offen offset:2048 sc1 lds\n"
"buffer_load_dwordx4 v1, s[52:55], s76 offen offset:3072 sc1 lds\n"
"s_nop 3\n"
"v_cvt_pk_bf16_f32 v140, v56, v57\n"
"v_cvt_pk_bf16_f32 v141, v58, v59\n"
"v_cvt_pk_bf16_f32 v142, v60, v61\n"
"v_cvt_pk_bf16_f32 v143, v62, v63\n"
"v_min_u32_e64 v140, -2, v140\n"
"v_min_u32_e64 v141, -2, v141\n"
"v_min_u32_e64 v142, -2, v142\n"
"v_min_u32_e64 v143, -2, v143\n"
"s_nop 0\n"
"v_permlane16_swap_b32 v140, v142\n"
"v_permlane16_swap_b32 v141, v143\n"
"buffer_store_dwordx4 v[140:143], v23, s[52:55], s16 offen sc1\n"
"v_lshlrev_b32_e32 v144, 16, v240\n"
"v_and_b32_e32 v145, 0xffff0000, v240\n"
"v_lshlrev_b32_e32 v146, 16, v241\n"
"v_and_b32_e32 v147, 0xffff0000, v241\n"
"v_lshlrev_b32_e32 v148, 16, v242\n"
"v_and_b32_e32 v149, 0xffff0000, v242\n"
"v_lshlrev_b32_e32 v150, 16, v243\n"
"v_and_b32_e32 v151, 0xffff0000, v243\n"
"v_lshlrev_b32_e32 v152, 16, v244\n"
"v_and_b32_e32 v153, 0xffff0000, v244\n"
"v_lshlrev_b32_e32 v154, 16, v245\n"
"v_and_b32_e32 v155, 0xffff0000, v245\n"
"v_lshlrev_b32_e32 v156, 16, v246\n"
"v_and_b32_e32 v157, 0xffff0000, v246\n"
"v_lshlrev_b32_e32 v158, 16, v247\n"
"v_and_b32_e32 v159, 0xffff0000, v247\n"
"s_waitcnt vmcnt(1)\n"
".Lcn_L_hv0:\n"
"ds_read_b128 v[160:163], v11 offset:16384\n"
"ds_read_b128 v[164:167], v11 offset:17408\n"
"ds_read_b128 v[168:171], v11 offset:18432\n"
"ds_read_b128 v[172:175], v11 offset:19456\n"
"ds_read_b128 v[176:179], v11 offset:32768\n"
"ds_read_b128 v[180:183], v11 offset:33792\n"
"ds_read_b128 v[184:187], v11 offset:34816\n"
"ds_read_b128 v[188:191], v11 offset:35840\n"
"s_waitcnt lgkmcnt(0)\n"
"v_max3_u32 v248, v160, v161, v162\n"
"v_max3_u32 v249, v163, v164, v165\n"
"v_max3_u32 v248, v248, v166, v167\n"
"v_max3_u32 v249, v249, v168, v169\n"
"v_max3_u32 v248, v248, v170, v171\n"
"v_max3_u32 v249, v249, v172, v173\n"
"v_max3_u32 v248, v248, v174, v175\n"
"v_max3_u32 v249, v249, v176, v177\n"
"v_max3_u32 v248, v248, v178, v179\n"
"v_max3_u32 v249, v249, v180, v181\n"
"v_max3_u32 v248, v248, v182, v183\n"
"v_max3_u32 v249, v249, v184, v185\n"
"v_max3_u32 v248, v248, v186, v187\n"
"v_max3_u32 v249, v249, v188, v189\n"
"v_max3_u32 v248, v248, v190, v191\n"
"v_max_u32_e64 v248, v248, v249\n"
"v_cmp_eq_u32_e64 vcc, -1, v248\n"
"s_cbranch_vccnz .Lcn_R_h0\n"
".Lcn_L_h0ok:\n"
"s_barrier\n"
"ds_read_b128 v[64:67], v29 offset:0\n"
"ds_read_b128 v[68:71], v29 offset:1024\n"
"ds_read_b128 v[72:75], v29 offset:2048\n"
"ds_read_b128 v[76:79], v29 offset:3072\n"
"ds_read_b128 v[80:83], v29 offset:4096\n"
"ds_read_b128 v[84:87], v29 offset:5120\n"
".p2align 3\n"
"s_waitcnt lgkmcnt(5)\n"
"v_mfma_f32_16x16x32_bf16 v[32:35], a[144:147], v[64:67], v[144:147]\n"
"ds_read_b128 v[88:91], v29 offset:6144\n"
"s_waitcnt lgkmcnt(5)\n"
"v_mfma_f32_16x16x32_bf16 v[32:35], a[152:155], v[68:71], v[32:35]\n"
"ds_read_b128 v[92:95], v29 offset:7168\n"
"s_waitcnt lgkmcnt(5)\n"
"v_mfma_f32_16x16x32_bf16 v[32:35], a[160:163], v[72:75], v[32:35]\n"
"ds_read_b128 v[96:99], v29 offset:8192\n"
"s_waitcnt lgkmcnt(5)\n"
"v_mfma_f32_16x16x32_bf16 v[32:35], a[168:171], v[76:79], v[32:35]\n"
"ds_read_b128 v[100:103], v29 offset:9216\n"
"s_waitcnt lgkmcnt(5)\n"
"v_mfma_f32_16x16x32_bf16 v[32:35], a[176:179], v[80:83], v[32:35]\n"
"ds_read_b128 v[104:107], v29 offset:10240\n"
"s_waitcnt lgkmcnt(5)\n"
"v_mfma_f32_16x16x32_bf16 v[32:35], a[184:187], v[84:87], v[32:35]\n"
"ds_read_b128 v[108:111], v29 offset:11264\n"
"s_waitcnt lgkmcnt(5)\n"
"v_mfma_f32_16x16x32_bf16 v[32:35], a[192:195], v[88:91], v[32:35]\n"
"ds_read_b128 v[112:115], v29 offset:12288\n"
"s_waitcnt lgkmcnt(5)\n"
"v_mfma_f32_16x16x32_bf16 v[32:35], a[200:203], v[92:95], v[32:35]\n"
"ds_read_b128 v[116:119], v29 offset:13312\n"
"s_waitcnt lgkmcnt(5)\n"
"v_mfma_f32_16x16x32_bf16 v[32:35], a[208:211], v[96:99], v[32:35]\n"
"ds_read_b128 v[120:123], v29 offset:14336\n"
"s_waitcnt lgkmcnt(5)\n"
"v_mfma_f32_16x16x32_bf16 v[32:35], a[216:219], v[100:103], v[32:35]\n"
"ds_read_b128 v[124:127], v29 offset:15360\n"
"s_waitcnt lgkmcnt(5)\n"
"v_mfma_f32_16x16x32_bf16 v[32:35], a[224:227], v[104:107], v[32:35]\n"
"s_add_u32 m0, s13, 114688\n"
"s_waitcnt lgkmcnt(4)\n"
"v_mfma_f32_16x16x32_bf16 v[32:35], a[232:235], v[108:111], v[32:35]\n"
"s_add_u32 s76, s17, 32768\n"
"s_waitcnt lgkmcnt(3)\n"
"v_mfma_f32_16x16x32_bf16 v[32:35], a[240:243], v[112:115], v[32:35]\n"
"buffer_load_dwordx4 v1, s[52:55], s76 offen offset:0 sc1 lds\n"
"s_waitcnt lgkmcnt(2)\n"
"v_mfma_f32_16x16x32_bf16 v[32:35], a[248:251], v[116:119], v[32:35]\n"
"buffer_load_dwordx4 v1, s[52:55], s76 offen offset:1024 sc1 lds\n"
"s_waitcnt lgkmcnt(1)\n"
"v_mfma_f32_16x16x32_bf16 v[32:35], v[208:211], v[120:123], v[32:35]\n"
"buffer_load_dwordx4 v1, s[52:55], s76 offen offset:2048 sc1 lds\n"
"s_waitcnt lgkmcnt(0)\n"
"v_mfma_f32_16x16x32_bf16 v[32:35], v[216:219], v[124:127], v[32:35]\n"
"buffer_load_dwordx4 v1, s[52:55], s76 offen offset:3072 sc1 lds\n"
"v_mfma_f32_16x16x32_bf16 v[36:39], a[148:151], v[64:67], v[148:151]\n"
"s_add_u32 m0, s13, 131072\n"
"v_mfma_f32_16x16x32_bf16 v[36:39], a[156:159], v[68:71], v[36:39]\n"
"s_add_u32 s76, s17, 49152\n"
"v_mfma_f32_16x16x32_bf16 v[36:39], a[164:167], v[72:75], v[36:39]\n"
"buffer_load_dwordx4 v1, s[52:55], s76 offen offset:0 sc1 lds\n"
"v_mfma_f32_16x16x32_bf16 v[36:39], a[172:175], v[76:79], v[36:39]\n"
"buffer_load_dwordx4 v1, s[52:55], s76 offen offset:1024 sc1 lds\n"
"v_mfma_f32_16x16x32_bf16 v[36:39], a[180:183], v[80:83], v[36:39]\n"
"buffer_load_dwordx4 v1, s[52:55], s76 offen offset:2048 sc1 lds\n"
"v_mfma_f32_16x16x32_bf16 v[36:39], a[188:191], v[84:87], v[36:39]\n"
"buffer_load_dwordx4 v1, s[52:55], s76 offen offset:3072 sc1 lds\n"
"v_mfma_f32_16x16x32_bf16 v[36:39], a[196:199], v[88:91], v[36:39]\n"
"v_mfma_f32_16x16x32_bf16 v[36:39], a[204:207], v[92:95], v[36:39]\n"
"v_mfma_f32_16x16x32_bf16 v[36:39], a[212:215], v[96:99], v[36:39]\n"
"v_mfma_f32_16x16x32_bf16 v[36:39], a[220:223], v[100:103], v[36:39]\n"
"v_mfma_f32_16x16x32_bf16 v[36:39], a[228:231], v[104:107], v[36:39]\n"
"v_mfma_f32_16x16x32_bf16 v[36:39], a[236:239], v[108:111], v[36:39]\n"
"v_mfma_f32_16x16x32_bf16 v[36:39], a[244:247], v[112:115], v[36:39]\n"
"v_mfma_f32_16x16x32_bf16 v[36:39], a[252:255], v[116:119], v[36:39]\n"
"v_mfma_f32_16x16x32_bf16 v[36:39], v[212:215], v[120:123], v[36:39]\n"
"v_mfma_f32_16x16x32_bf16 v[36:39], v[220:223], v[124:127], v[36:39]\n"
"v_cvt_pk_bf16_f32 v136, v32, v33\n"
"v_cvt_pk_bf16_f32 v137, v34, v35\n"
"s_nop 5\n"
"v_cvt_pk_bf16_f32 v138, v36, v37\n"
"v_cvt_pk_bf16_f32 v139, v38, v39\n"
"buffer_store_dwordx4 v[136:139], v24, s[48:51], s14 offen sc1\n"
"s_waitcnt vmcnt(1)\n"
".Lcn_L_hv1:\n"
"ds_read_b128 v[160:163], v30 offset:0\n"
"ds_read_b128 v[164:167], v30 offset:1024\n"
"ds_read_b128 v[168:171], v30 offset:2048\n"
"ds_read_b128 v[172:175], v30 offset:3072\n"
"ds_read_b128 v[176:179], v30 offset:16384\n"
"ds_read_b128 v[180:183], v30 offset:17408\n"
"ds_read_b128 v[184:187], v30 offset:18432\n"
"ds_read_b128 v[188:191], v30 offset:19456\n"
"s_waitcnt lgkmcnt(0)\n"
"v_max3_u32 v248, v160, v161, v162\n"
"v_max3_u32 v249, v163, v164, v165\n"
"v_max3_u32 v248, v248, v166, v167\n"
"v_max3_u32 v249, v249, v168, v169\n"
"v_max3_u32 v248, v248, v170, v171\n"
"v_max3_u32 v249, v249, v172, v173\n"
"v_max3_u32 v248, v248, v174, v175\n"
"v_max3_u32 v249, v249, v176, v177\n"
"v_max3_u32 v248, v248, v178, v179\n"
"v_max3_u32 v249, v249, v180, v181\n"
"v_max3_u32 v248, v248, v182, v183\n"
"v_max3_u32 v249, v249, v184, v185\n"
"v_max3_u32 v248, v248, v186, v187\n"
"v_max3_u32 v249, v249, v188, v189\n"
"v_max3_u32 v248, v248, v190, v191\n"
"v_max_u32_e64 v248, v248, v249\n"
"v_cmp_eq_u32_e64 vcc, -1, v248\n"
"s_cbranch_vccnz .Lcn_R_h1\n"
".Lcn_L_h1ok:\n"
"s_barrier\n"
"buffer_store_dword v251, v26, s[52:55], 0 offen sc1\n"
"ds_read_b128 v[64:67], v29 offset:32768\n"
"ds_read_b128 v[68:71], v29 offset:33792\n"
"ds_read_b128 v[72:75], v29 offset:34816\n"
"ds_read_b128 v[76:79], v29 offset:35840\n"
"ds_read_b128 v[80:83], v29 offset:36864\n"
"ds_read_b128 v[84:87], v29 offset:37888\n"
".p2align 3\n"
"s_waitcnt lgkmcnt(5)\n"
"v_mfma_f32_16x16x32_bf16 v[40:43], a[144:147], v[64:67], v[152:155]\n"
"ds_read_b128 v[88:91], v29 offset:38912\n"
"s_waitcnt lgkmcnt(5)\n"
"v_mfma_f32_16x16x32_bf16 v[40:43], a[152:155], v[68:71], v[40:43]\n"
"ds_read_b128 v[92:95], v29 offset:39936\n"
"s_waitcnt lgkmcnt(5)\n"
"v_mfma_f32_16x16x32_bf16 v[40:43], a[160:163], v[72:75], v[40:43]\n"
"ds_read_b128 v[96:99], v29 offset:40960\n"
"s_waitcnt lgkmcnt(5)\n"
"v_mfma_f32_16x16x32_bf16 v[40:43], a[168:171], v[76:79], v[40:43]\n"
"ds_read_b128 v[100:103], v29 offset:41984\n"
"s_waitcnt lgkmcnt(5)\n"
"v_mfma_f32_16x16x32_bf16 v[40:43], a[176:179], v[80:83], v[40:43]\n"
"ds_read_b128 v[104:107], v29 offset:43008\n"
"s_waitcnt lgkmcnt(5)\n"
"v_mfma_f32_16x16x32_bf16 v[40:43], a[184:187], v[84:87], v[40:43]\n"
"ds_read_b128 v[108:111], v29 offset:44032\n"
"s_waitcnt lgkmcnt(5)\n"
"v_mfma_f32_16x16x32_bf16 v[40:43], a[192:195], v[88:91], v[40:43]\n"
"ds_read_b128 v[112:115], v29 offset:45056\n"
"s_waitcnt lgkmcnt(5)\n"
"v_mfma_f32_16x16x32_bf16 v[40:43], a[200:203], v[92:95], v[40:43]\n"
"ds_read_b128 v[116:119], v29 offset:46080\n"
"s_waitcnt lgkmcnt(5)\n"
"v_mfma_f32_16x16x32_bf16 v[40:43], a[208:211], v[96:99], v[40:43]\n"
"ds_read_b128 v[120:123], v29 offset:47104\n"
"s_waitcnt lgkmcnt(5)\n"
"v_mfma_f32_16x16x32_bf16 v[40:43], a[216:219], v[100:103], v[40:43]\n"
"ds_read_b128 v[124:127], v29 offset:48128\n"
"s_waitcnt lgkmcnt(5)\n"
"v_mfma_f32_16x16x32_bf16 v[40:43], a[224:227], v[104:107], v[40:43]\n"
"s_waitcnt lgkmcnt(4)\n"
"v_mfma_f32_16x16x32_bf16 v[40:43], a[232:235], v[108:111], v[40:43]\n"
"s_waitcnt lgkmcnt(3)\n"
"v_mfma_f32_16x16x32_bf16 v[40:43], a[240:243], v[112:115], v[40:43]\n"
"s_waitcnt lgkmcnt(2)\n"
"v_mfma_f32_16x16x32_bf16 v[40:43], a[248:251], v[116:119], v[40:43]\n"
"s_waitcnt lgkmcnt(1)\n"
"v_mfma_f32_16x16x32_bf16 v[40:43], v[208:211], v[120:123], v[40:43]\n"
"s_waitcnt lgkmcnt(0)\n"
"v_mfma_f32_16x16x32_bf16 v[40:43], v[216:219], v[124:127], v[40:43]\n"
"v_mfma_f32_16x16x32_bf16 v[44:47], a[148:151], v[64:67], v[156:159]\n"
"v_mfma_f32_16x16x32_bf16 v[44:47], a[156:159], v[68:71], v[44:47]\n"
"v_mfma_f32_16x16x32_bf16 v[44:47], a[164:167], v[72:75], v[44:47]\n"
"v_mfma_f32_16x16x32_bf16 v[44:47], a[172:175], v[76:79], v[44:47]\n"
"v_mfma_f32_16x16x32_bf16 v[44:47], a[180:183], v[80:83], v[44:47]\n"
"v_mfma_f32_16x16x32_bf16 v[44:47], a[188:191], v[84:87], v[44:47]\n"
"v_mfma_f32_16x16x32_bf16 v[44:47], a[196:199], v[88:91], v[44:47]\n"
"v_mfma_f32_16x16x32_bf16 v[44:47], a[204:207], v[92:95], v[44:47]\n"
"v_mfma_f32_16x16x32_bf16 v[44:47], a[212:215], v[96:99], v[44:47]\n"
"v_mfma_f32_16x16x32_bf16 v[44:47], a[220:223], v[100:103], v[44:47]\n"
"v_mfma_f32_16x16x32_bf16 v[44:47], a[228:231], v[104:107], v[44:47]\n"
"v_mfma_f32_16x16x32_bf16 v[44:47], a[236:239], v[108:111], v[44:47]\n"
"v_mfma_f32_16x16x32_bf16 v[44:47], a[244:247], v[112:115], v[44:47]\n"
"v_mfma_f32_16x16x32_bf16 v[44:47], a[252:255], v[116:119], v[44:47]\n"
"v_mfma_f32_16x16x32_bf16 v[44:47], v[212:215], v[120:123], v[44:47]\n"
"v_mfma_f32_16x16x32_bf16 v[44:47], v[220:223], v[124:127], v[44:47]\n"
"v_cvt_pk_bf16_f32 v140, v40, v41\n"
"v_cvt_pk_bf16_f32 v141, v42, v43\n"
"s_nop 5\n"
"v_cvt_pk_bf16_f32 v142, v44, v45\n"
"v_cvt_pk_bf16_f32 v143, v46, v47\n"
"buffer_store_dwordx4 v[140:143], v24, s[48:51], s80 offen sc1\n"
"s_branch .Lcn_L_end\n"
".Lcn_R_st:\n"
"s_nop 7\n"
"s_add_u32 s82, s82, 1\n"
"s_cmp_gt_u32 s82, 32768\n"
"s_cbranch_scc1 .Lcn_L_stok\n"
"buffer_load_dwordx4 v[64:67], v1, s[52:55], s7 offen sc1\n"
"buffer_load_dwordx4 v[68:71], v1, s[52:55], s7 offen offset:1024 sc1\n"
"s_waitcnt vmcnt(0)\n"
"s_nop 4\n"
"s_branch .Lcn_L_stchk\n"
".Lcn_R_tk:\n"
"s_nop 7\n"
"s_add_u32 s82, s82, 1\n"
"s_cmp_gt_u32 s82, 32768\n"
"s_cbranch_scc1 .Lcn_L_tkok\n"
"buffer_load_dword v72, v17, s[52:55], 0 offen sc1\n"
"s_waitcnt vmcnt(0)\n"
"s_nop 4\n"
"s_branch .Lcn_L_tkchk\n"
".Lcn_R_a0:\n"
"s_nop 7\n"
"s_add_u32 s82, s82, 1\n"
"s_cmp_gt_u32 s82, 32768\n"
"s_cbranch_scc1 .Lcn_L_a0ok\n"
"s_add_u32 m0, s13, 0\n"
"s_add_u32 s85, s24, 32768\n"
"buffer_load_dwordx4 v1, s[28:31], s24 offen sc1 lds\n"
"buffer_load_dwordx4 v2, s[28:31], s24 offen offset:1024 sc1 lds\n"
"s_add_u32 m0, s13, 2048\n"
"s_nop 0\n"
"buffer_load_dwordx4 v1, s[28:31], s85 offen sc1 lds\n"
"buffer_load_dwordx4 v2, s[28:31], s85 offen offset:1024 sc1 lds\n"
"s_add_u32 m0, s13, 16384\n"
"s_add_u32 s85, s25, 32768\n"
"buffer_load_dwordx4 v1, s[28:31], s25 offen sc1 lds\n"
"buffer_load_dwordx4 v2, s[28:31], s25 offen offset:1024 sc1 lds\n"
"s_add_u32 m0, s13, 18432\n"
"s_nop 0\n"
"buffer_load_dwordx4 v1, s[28:31], s85 offen sc1 lds\n"
"buffer_load_dwordx4 v2, s[28:31], s85 offen offset:1024 sc1 lds\n"
"s_waitcnt vmcnt(0)\n"
"s_nop 4\n"
"s_branch .Lcn_L_a0chk\n"
".Lcn_R_a2:\n"
"s_nop 7\n"
"s_add_u32 s82, s82, 1\n"
"s_cmp_gt_u32 s82, 32768\n"
"s_cbranch_scc1 .Lcn_L_a2ok\n"
"s_mov_b32 s86, m0\n"
"s_add_u32 m0, s13, 32768\n"
"s_add_u32 s85, s26, 32768\n"
"buffer_load_dwordx4 v1, s[28:31], s26 offen sc1 lds\n"
"buffer_load_dwordx4 v2, s[28:31], s26 offen offset:1024 sc1 lds\n"
"s_add_u32 m0, s13, 34816\n"
"s_nop 0\n"
"buffer_load_dwordx4 v1, s[28:31], s85 offen sc1 lds\n"
"buffer_load_dwordx4 v2, s[28:31], s85 offen offset:1024 sc1 lds\n"
"s_mov_b32 m0, s86\n"
"s_waitcnt vmcnt(0)\n"
"ds_read_b128 v[160:163], v10 offset:32768\n"
"ds_read_b128 v[164:167], v10 offset:33792\n"
"ds_read_b128 v[168:171], v10 offset:34816\n"
"ds_read_b128 v[172:175], v10 offset:35840\n"
"s_waitcnt lgkmcnt(0)\n"
"v_max3_u32 v248, v160, v161, v162\n"
"v_max3_u32 v249, v163, v164, v165\n"
"v_max3_u32 v248, v248, v166, v167\n"
"v_max3_u32 v249, v249, v168, v169\n"
"v_max3_u32 v248, v248, v170, v171\n"
"v_max3_u32 v249, v249, v172, v173\n"
"v_max3_u32 v248, v248, v174, v175\n"
"v_max_u32_e64 v248, v248, v249\n"
"v_cmp_eq_u32_e64 vcc, -1, v248\n"
"s_cbranch_vccnz .Lcn_R_a2\n"
"s_nop 4\n"
"s_branch .Lcn_L_a2ok\n"
".Lcn_R_a3:\n"
"s_nop 7\n"
"s_add_u32 s82, s82, 1\n"
"s_cmp_gt_u32 s82, 32768\n"
"s_cbranch_scc1 .Lcn_L_a3ok\n"
"s_mov_b32 s86, m0\n"
"s_add_u32 m0, s13, 49152\n"
"s_add_u32 s85, s27, 32768\n"
"buffer_load_dwordx4 v1, s[28:31], s27 offen sc1 lds\n"
"buffer_load_dwordx4 v2, s[28:31], s27 offen offset:1024 sc1 lds\n"
"s_add_u32 m0, s13, 51200\n"
"s_nop 0\n"
"buffer_load_dwordx4 v1, s[28:31], s85 offen sc1 lds\n"
"buffer_load_dwordx4 v2, s[28:31], s85 offen offset:1024 sc1 lds\n"
"s_mov_b32 m0, s86\n"
"s_waitcnt vmcnt(0)\n"
"ds_read_b128 v[160:163], v10 offset:49152\n"
"ds_read_b128 v[164:167], v10 offset:50176\n"
"ds_read_b128 v[168:171], v10 offset:51200\n"
"ds_read_b128 v[172:175], v10 offset:52224\n"
"s_waitcnt lgkmcnt(0)\n"
"v_max3_u32 v248, v160, v161, v162\n"
"v_max3_u32 v249, v163, v164, v165\n"
"v_max3_u32 v248, v248, v166, v167\n"
"v_max3_u32 v249, v249, v168, v169\n"
"v_max3_u32 v248, v248, v170, v171\n"
"v_max3_u32 v249, v249, v172, v173\n"
"v_max3_u32 v248, v248, v174, v175\n"
"v_max_u32_e64 v248, v248, v249\n"
"v_cmp_eq_u32_e64 vcc, -1, v248\n"
"s_cbranch_vccnz .Lcn_R_a3\n"
"s_nop 4\n"
"s_branch .Lcn_L_a3ok\n"
".Lcn_R_a4:\n"
"s_nop 7\n"
"s_add_u32 s82, s82, 1\n"
"s_cmp_gt_u32 s82, 32768\n"
"s_cbranch_scc1 .Lcn_L_a4ok\n"
"s_mov_b32 s86, m0\n"
"s_add_u32 m0, s13, 65536\n"
"s_add_u32 s85, s32, 32768\n"
"buffer_load_dwordx4 v1, s[28:31], s32 offen sc1 lds\n"
"buffer_load_dwordx4 v2, s[28:31], s32 offen offset:1024 sc1 lds\n"
"s_add_u32 m0, s13, 67584\n"
"s_nop 0\n"
"buffer_load_dwordx4 v1, s[28:31], s85 offen sc1 lds\n"
"buffer_load_dwordx4 v2, s[28:31], s85 offen offset:1024 sc1 lds\n"
"s_mov_b32 m0, s86\n"
"s_waitcnt vmcnt(0)\n"
"ds_read_b128 v[160:163], v11 offset:0\n"
"ds_read_b128 v[164:167], v11 offset:1024\n"
"ds_read_b128 v[168:171], v11 offset:2048\n"
"ds_read_b128 v[172:175], v11 offset:3072\n"
"s_waitcnt lgkmcnt(0)\n"
"v_max3_u32 v248, v160, v161, v162\n"
"v_max3_u32 v249, v163, v164, v165\n"
"v_max3_u32 v248, v248, v166, v167\n"
"v_max3_u32 v249, v249, v168, v169\n"
"v_max3_u32 v248, v248, v170, v171\n"
"v_max3_u32 v249, v249, v172, v173\n"
"v_max3_u32 v248, v248, v174, v175\n"
"v_max_u32_e64 v248, v248, v249\n"
"v_cmp_eq_u32_e64 vcc, -1, v248\n"
"s_cbranch_vccnz .Lcn_R_a4\n"
"s_nop 4\n"
"s_branch .Lcn_L_a4ok\n"
".Lcn_R_x0:\n"
"s_nop 7\n"
"s_add_u32 s82, s82, 1\n"
"s_cmp_gt_u32 s82, 32768\n"
"s_cbranch_scc1 .Lcn_L_x0ok\n"
"s_add_u32 s85, s11, 2097152\n"
"buffer_load_dwordx4 v[164:167], v1, s[52:55], s85 offen offset:0 sc1\n"
"s_add_u32 s85, s11, 4194304\n"
"buffer_load_dwordx4 v[168:171], v1, s[52:55], s85 offen offset:0 sc1\n"
"s_waitcnt vmcnt(0)\n"
"v_max3_u32 v248, v164, v165, v166\n"
"v_max3_u32 v249, v167, v168, v169\n"
"v_max3_u32 v248, v248, v170, v171\n"
"v_max_u32_e64 v248, v248, v249\n"
"v_cmp_eq_u32_e64 vcc, -1, v248\n"
"s_cbranch_vccnz .Lcn_R_x0\n"
"s_nop 4\n"
"s_branch .Lcn_L_x0ok\n"
".Lcn_R_x1:\n"
"s_nop 7\n"
"s_add_u32 s82, s82, 1\n"
"s_cmp_gt_u32 s82, 32768\n"
"s_cbranch_scc1 .Lcn_L_x1ok\n"
"s_add_u32 s85, s11, 2097152\n"
"buffer_load_dwordx4 v[176:179], v1, s[52:55], s85 offen offset:1024 sc1\n"
"s_add_u32 s85, s11, 4194304\n"
"buffer_load_dwordx4 v[180:183], v1, s[52:55], s85 offen offset:1024 sc1\n"
"s_waitcnt vmcnt(0)\n"
"v_max3_u32 v248, v176, v177, v178\n"
"v_max3_u32 v249, v179, v180, v181\n"
"v_max3_u32 v248, v248, v182, v183\n"
"v_max_u32_e64 v248, v248, v249\n"
"v_cmp_eq_u32_e64 vcc, -1, v248\n"
"s_cbranch_vccnz .Lcn_R_x1\n"
"s_nop 4\n"
"s_branch .Lcn_L_x1ok\n"
".Lcn_R_t2:\n"
"s_nop 7\n"
"s_add_u32 s82, s82, 1\n"
"s_cmp_gt_u32 s82, 32768\n"
"s_cbranch_scc1 .Lcn_L_t2ok\n"
"buffer_load_dword v254, v17, s[52:55], s79 offen sc1\n"
"s_waitcnt vmcnt(0)\n"
"v_cmp_ne_u32_e32 vcc, s66, v254\n"
"s_and_b64 vcc, vcc, 0x7ff00000\n"
"s_cbranch_vccnz .Lcn_R_t2\n"
"s_nop 4\n"
"s_branch .Lcn_L_t2ok\n"
".Lcn_R_h0:\n"
"s_nop 7\n"
"s_add_u32 s82, s82, 1\n"
"s_cmp_gt_u32 s82, 32768\n"
"s_cbranch_scc1 .Lcn_L_h0ok\n"
"s_add_u32 m0, s13, 81920\n"
"s_add_u32 s85, s17, 0\n"
"buffer_load_dwordx4 v1, s[52:55], s85 offen offset:0 sc1 lds\n"
"buffer_load_dwordx4 v1, s[52:55], s85 offen offset:1024 sc1 lds\n"
"buffer_load_dwordx4 v1, s[52:55], s85 offen offset:2048 sc1 lds\n"
"buffer_load_dwordx4 v1, s[52:55], s85 offen offset:3072 sc1 lds\n"
"s_add_u32 m0, s13, 98304\n"
"s_add_u32 s85, s17, 16384\n"
"buffer_load_dwordx4 v1, s[52:55], s85 offen offset:0 sc1 lds\n"
"buffer_load_dwordx4 v1, s[52:55], s85 offen offset:1024 sc1 lds\n"
"buffer_load_dwordx4 v1, s[52:55], s85 offen offset:2048 sc1 lds\n"
"buffer_load_dwordx4 v1, s[52:55], s85 offen offset:3072 sc1 lds\n"
"s_waitcnt vmcnt(0)\n"
"s_nop 4\n"
"s_branch .Lcn_L_hv0\n"
".Lcn_R_h1:\n"
"s_nop 7\n"
"s_add_u32 s82, s82, 1\n"
"s_cmp_gt_u32 s82, 32768\n"
"s_cbranch_scc1 .Lcn_L_h1ok\n"
"s_add_u32 m0, s13, 114688\n"
"s_add_u32 s85, s17, 32768\n"
"buffer_load_dwordx4 v1, s[52:55], s85 offen offset:0 sc1 lds\n"
"buffer_load_dwordx4 v1, s[52:55], s85 offen offset:1024 sc1 lds\n"
"buffer_load_dwordx4 v1, s[52:55], s85 offen offset:2048 sc1 lds\n"
"buffer_load_dwordx4 v1, s[52:55], s85 offen offset:3072 sc1 lds\n"
"s_add_u32 m0, s13, 131072\n"
"s_add_u32 s85, s17, 49152\n"
"buffer_load_dwordx4 v1, s[52:55], s85 offen offset:0 sc1 lds\n"
"buffer_load_dwordx4 v1, s[52:55], s85 offen offset:1024 sc1 lds\n"
"buffer_load_dwordx4 v1, s[52:55], s85 offen offset:2048 sc1 lds\n"
"buffer_load_dwordx4 v1, s[52:55], s85 offen offset:3072 sc1 lds\n"
"s_waitcnt vmcnt(0)\n"
"s_nop 4\n"
"s_branch .Lcn_L_hv1\n"
".Lcn_L_end:\n"
  :: [ka] "s"(__builtin_amdgcn_kernarg_segment_ptr()), [wg] "s"(__builtin_amdgcn_workgroup_id_x()), [tid] "v"(__builtin_amdgcn_workitem_id_x()),
     [lds] "s"((unsigned)(size_t)lds)
  : "v1","v2","v3","v4","v5","v6","v7","v8","v9","v10","v11","v12","v13","v14","v15","v16","v17","v18","v19","v20","v21","v22","v23","v24","v25","v26","v27","v28","v29","v30","v31","v32","v33","v34","v35","v36","v37","v38","v39","v40","v41","v42","v43","v44","v45","v46","v47","v48","v49","v50","v51","v52","v53","v54","v55","v56","v57","v58","v59","v60","v61","v62","v63","v64","v65","v66","v67","v68","v69","v70","v71","v72","v73","v74","v75","v76","v77","v78","v79","v80","v81","v82","v83","v84","v85","v86","v87","v88","v89","v90","v91","v92","v93","v94","v95","v96","v97","v98","v99","v100","v101","v102","v103","v104","v105","v106","v107","v108","v109","v110","v111","v112","v113","v114","v115","v116","v117","v118","v119","v120","v121","v122","v123","v124","v125","v126","v127","v128","v129","v130","v131","v132","v133","v134","v135","v136","v137","v138","v139","v140","v141","v142","v143","v144","v145","v146","v147","v148","v149","v150","v151","v152","v153","v154","v155","v156","v157","v158","v159","v160","v161","v162","v163","v164","v165","v166","v167","v168","v169","v170","v171","v172","v173","v174","v175","v176","v177","v178","v179","v180","v181","v182","v183","v184","v185","v186","v187","v188","v189","v190","v191","v192","v193","v194","v195","v196","v197","v198","v199","v200","v201","v202","v203","v204","v205","v206","v207","v208","v209","v210","v211","v212","v213","v214","v215","v216","v217","v218","v219","v220","v221","v222","v223","v224","v225","v226","v227","v228","v229","v230","v231","v232","v233","v234","v235","v236","v237","v238","v239","v240","v241","v242","v243","v244","v245","v246","v247","v248","v249","v250","v251","v252","v253","v254","v255","a0","a1","a2","a3","a4","a5","a6","a7","a8","a9","a10","a11","a12","a13","a14","a15","a16","a17","a18","a19","a20","a21","a22","a23","a24","a25","a26","a27","a28","a29","a30","a31","a32","a33","a34","a35","a36","a37","a38","a39","a40","a41","a42","a43","a44","a45","a46","a47","a48","a49","a50","a51","a52","a53","a54","a55","a56","a57","a58","a59","a60","a61","a62","a63","a64","a65","a66","a67","a68","a69","a70","a71","a72","a73","a74","a75","a76","a77","a78","a79","a80","a81","a82","a83","a84","a85","a86","a87","a88","a89","a90","a91","a92","a93","a94","a95","a96","a97","a98","a99","a100","a101","a102","a103","a104","a105","a106","a107","a108","a109","a110","a111","a112","a113","a114","a115","a116","a117","a118","a119","a120","a121","a122","a123","a124","a125","a126","a127","a128","a129","a130","a131","a132","a133","a134","a135","a136","a137","a138","a139","a140","a141","a142","a143","a144","a145","a146","a147","a148","a149","a150","a151","a152","a153","a154","a155","a156","a157","a158","a159","a160","a161","a162","a163","a164","a165","a166","a167","a168","a169","a170","a171","a172","a173","a174","a175","a176","a177","a178","a179","a180","a181","a182","a183","a184","a185","a186","a187","a188","a189","a190","a191","a192","a193","a194","a195","a196","a197","a198","a199","a200","a201","a202","a203","a204","a205","a206","a207","a208","a209","a210","a211","a212","a213","a214","a215","a216","a217","a218","a219","a220","a221","a222","a223","a224","a225","a226","a227","a228","a229","a230","a231","a232","a233","a234","a235","a236","a237","a238","a239","a240","a241","a242","a243","a244","a245","a246","a247","a248","a249","a250","a251","a252","a253","a254","a255","s4","s5","s6","s7","s8","s9","s10","s11","s12","s13","s14","s15","s16","s17","s18","s19","s20","s21","s22","s23","s24","s25","s26","s27","s28","s29","s30","s31","s32","s33","s34","s35","s36","s37","s38","s39","s40","s41","s42","s43","s44","s45","s46","s47","s48","s49","s50","s51","s52","s53","s54","s55","s56","s57","s58","s59","s60","s61","s62","s63","s64","s65","s66","s67","s68","s69","s70","s71","s72","s73","s74","s75","s76","s77","s78","s79","s80","s81","s82","s83","s84","s85","s86","s87","s88","s89","s90","s91","s92","s93","s94","s95","s96","s97","s98","s99","s100","s101","vcc","scc","memory");
#endif
}

extern "C" void kernel_launch(void* const* d_in, const int* in_sizes, int n_in, void* d_out, int out_size, void* d_ws, size_t ws_size,
                              hipStream_t stream) {
  cn_fused<<<256, 256, 0, stream>>>(d_in[0], d_in[1], d_in[2], d_in[3], d_in[4], d_in[5], d_out, d_ws);
}
```
